```python
import torch

def _softmax(x, axis=-1):
    return torch.softmax(x, dim=axis)
def _gelu(x):
    return torch.nn.functional.gelu(x, approximate="tanh")
def _silu(x):
    return torch.nn.functional.silu(x)
def _sigmoid(x):
    return torch.sigmoid(x)
def _layernorm(x, axis=-1, eps=1e-5):
    m = x.mean(dim=axis, keepdim=True)
    v = x.var(dim=axis, keepdim=True, unbiased=False)
    return (x - m) / torch.sqrt(v + eps)
def _scatter_add(tgt, idx, src, axis=0):
    return tgt.clone().index_add_(axis, idx, src)
def _segment_sum(src, seg, n):
    out = torch.zeros((n,) + src.shape[1:], dtype=src.dtype)
    return out.index_add_(0, seg, src)
def _pad(x, pw):
    flat = tuple(v for pair in reversed(pw) for v in pair)
    return torch.nn.functional.pad(x, flat)

kernel_name = 'concat_heads__mha_block_cached__mha_block_cached__near_ridge'

def setup_inputs():

    _w = (lambda a: (lambda t: t.to(torch.bfloat16) if t.is_floating_point() else t)(torch.as_tensor(a)))
    def _A_setup():
        B, Sq, D = 8, 144, 640
        H, Sk, Dh = 32, 1024, 128
        return {
            "x": _w((lambda *s: torch.randn(*s, dtype=torch.bfloat16) * 0.8)(B, Sq, D)),
            "k_cache": _w((lambda *s: torch.randn(*s, dtype=torch.bfloat16) * 0.8)(B, H, Sk, Dh)),
            "v_cache": _w((lambda *s: torch.randn(*s, dtype=torch.bfloat16) * 0.05)(B, H, Sk, Dh)),
            "wq": _w((lambda *s: torch.randn(*s, dtype=torch.bfloat16) * 0.8)(D, H * Dh) * (D ** -0.5)),
            "wo": _w((lambda *s: torch.randn(*s, dtype=torch.bfloat16) * 0.05)(H * Dh, D) * ((H * Dh) ** -0.5)),
        }
    _A_in = _A_setup()

    def _B_setup():
        B, Sq, D = 8, 144, 640
        H, Sk, Dh = 32, 1024, 128
        return {
            "x": _w((lambda *s: torch.randn(*s, dtype=torch.bfloat16) * 0.8)(B, Sq, D)),
            "k_cache": _w((lambda *s: torch.randn(*s, dtype=torch.bfloat16) * 0.8)(B, H, Sk, Dh)),
            "v_cache": _w((lambda *s: torch.randn(*s, dtype=torch.bfloat16) * 0.05)(B, H, Sk, Dh)),
            "wq": _w((lambda *s: torch.randn(*s, dtype=torch.bfloat16) * 0.8)(D, H * Dh) * (D ** -0.5)),
            "wo": _w((lambda *s: torch.randn(*s, dtype=torch.bfloat16) * 0.05)(H * Dh, D) * ((H * Dh) ** -0.5)),
        }
    _B_in = _B_setup()

    return {
        "A_x": _A_in["x"],
        "A_k_cache": _A_in["k_cache"],
        "A_v_cache": _A_in["v_cache"],
        "A_wq": _A_in["wq"],
        "A_wo": _A_in["wo"],
        "B_x": _B_in["x"],
        "B_k_cache": _B_in["k_cache"],
        "B_v_cache": _B_in["v_cache"],
        "B_wq": _B_in["wq"],
        "B_wo": _B_in["wo"],
    }

def reference(A_x, A_k_cache, A_v_cache, A_wq, A_wo, B_x, B_k_cache, B_v_cache, B_wq, B_wo):
    def _A_ref(x, k_cache, v_cache, wq, wo):
        B, Sq, D = x.shape
        H, Dh = 32, 128
        q = (lambda x, *d: x.permute(*d))((x @ wq).reshape(B, Sq, H, Dh), 0, 2, 1, 3)
        scores = torch.einsum("bhqd,bhkd->bhqk", q, k_cache) * (Dh ** -0.5)
        p = _softmax(scores, axis=-1)
        o = torch.einsum("bhqk,bhkd->bhqd", p, v_cache)
        return (lambda x, *d: x.permute(*d))(o, 0, 2, 1, 3).reshape(B, Sq, H * Dh) @ wo

    def _B_ref(x, k_cache, v_cache, wq, wo):
        B, Sq, D = x.shape
        H, Dh = 32, 128
        q = (lambda x, *d: x.permute(*d))((x @ wq).reshape(B, Sq, H, Dh), 0, 2, 1, 3)
        scores = torch.einsum("bhqd,bhkd->bhqk", q, k_cache) * (Dh ** -0.5)
        p = _softmax(scores, axis=-1)
        o = torch.einsum("bhqk,bhkd->bhqd", p, v_cache)
        return (lambda x, *d: x.permute(*d))(o, 0, 2, 1, 3).reshape(B, Sq, H * Dh) @ wo

    a = _A_ref(A_x, A_k_cache, A_v_cache, A_wq, A_wo)
    b = _B_ref(B_x, B_k_cache, B_v_cache, B_wq, B_wo)
    return torch.concatenate([a, b], axis=-1)
```

```cpp
#include <hip/hip_runtime.h>
#include <hip/hip_bf16.h>
#include <stdint.h>

typedef short    s16x4 __attribute__((ext_vector_type(4)));
typedef short    s16x8 __attribute__((ext_vector_type(8)));
typedef float    f32x4 __attribute__((ext_vector_type(4)));
typedef float    f32x2 __attribute__((ext_vector_type(2)));
typedef __bf16   bf16x2 __attribute__((ext_vector_type(2)));
typedef unsigned u32x4 __attribute__((ext_vector_type(4)));
typedef unsigned u32x2 __attribute__((ext_vector_type(2)));
typedef __attribute__((address_space(3))) s16x4* lds_s16x4_ptr;
typedef __attribute__((address_space(3))) const s16x8* lds_s16x8_ptr;
#define DI __device__ __forceinline__

constexpr int NB = 8, SQ = 144, DM = 640, NH = 32, SK = 1024, DH = 128;
constexpr int HD = NH * DH;
constexpr int MROWS = NB * SQ;

DI unsigned cvtpk(float lo, float hi) {
  f32x2 v = {lo, hi};
  bf16x2 b = __builtin_convertvector(v, bf16x2);
  return __builtin_bit_cast(unsigned, b);
}
DI f32x4 mfma16(s16x8 a, s16x8 b, f32x4 c) {
  return __builtin_amdgcn_mfma_f32_16x16x32_bf16(a, b, c, 0, 0, 0);
}
DI void dma16(unsigned lds_addr, unsigned voff, const char* gbase) {
  asm volatile("s_mov_b32 m0, %0\n\ts_nop 0\n\tglobal_load_lds_dwordx4 %1, %2"
               :: "s"(lds_addr), "v"(voff), "s"(gbase) : "memory");
}
template <int N> DI void wait_vm() { asm volatile("s_waitcnt vmcnt(%0)" :: "i"(N) : "memory"); }
DI void bar() { asm volatile("s_barrier" ::: "memory"); }
DI s16x8 lds_b128(unsigned addr) { return *(lds_s16x8_ptr)(uintptr_t)addr; }
DI s16x8 lds_tr2(unsigned addr0, unsigned addr1) {
  s16x4 lo = __builtin_amdgcn_ds_read_tr16_b64_v4i16((lds_s16x4_ptr)(uintptr_t)addr0);
  s16x4 hi = __builtin_amdgcn_ds_read_tr16_b64_v4i16((lds_s16x4_ptr)(uintptr_t)addr1);
  return __builtin_shufflevector(lo, hi, 0, 1, 2, 3, 4, 5, 6, 7);
}
DI float xmax16_32(float v) {
  auto r = __builtin_amdgcn_permlane32_swap(__float_as_uint(v), __float_as_uint(v), false, false);
  v = fmaxf(__uint_as_float(r[0]), __uint_as_float(r[1]));
  auto r2 = __builtin_amdgcn_permlane16_swap(__float_as_uint(v), __float_as_uint(v), false, false);
  return fmaxf(__uint_as_float(r2[0]), __uint_as_float(r2[1]));
}
DI float xsum16_32(float v) {
  auto r = __builtin_amdgcn_permlane32_swap(__float_as_uint(v), __float_as_uint(v), false, false);
  v = __uint_as_float(r[0]) + __uint_as_float(r[1]);
  auto r2 = __builtin_amdgcn_permlane16_swap(__float_as_uint(v), __float_as_uint(v), false, false);
  return __uint_as_float(r2[0]) + __uint_as_float(r2[1]);
}

constexpr int K1_WAVES = 9;
constexpr int K1_NSLOT = 5;
constexpr int K1_TILE  = 16384;
constexpr int K1_NW    = 10;
constexpr int K1_NT    = K1_NW + 32;
constexpr int K1_LDS   = K1_NSLOT * K1_TILE;
constexpr float SC_L2E = 0.088388347648318447f * 1.4426950408889634f;

struct K1State {
  unsigned offK[2], offV[2];
  const char *kg, *vg;
  unsigned ldsw;
  unsigned aK, aV;
  s16x8 q[4];
  f32x4 o[8];
  float m, l;
  s16x8 p[2];
  int wave;
};

DI void k1_issue_kv(K1State& st, int j, int slot) {
  const int t = (j - K1_NW) >> 1;
  const unsigned lb = slot * K1_TILE + st.ldsw;
  if ((j - K1_NW) & 1) {
    const char* g = st.vg + (size_t)t * K1_TILE;
    dma16(lb, st.offV[0], g); dma16(lb + 1024, st.offV[1], g);
  } else {
    const char* g = st.kg + (size_t)t * K1_TILE;
    dma16(lb, st.offK[0], g); dma16(lb + 1024, st.offK[1], g);
  }
}

DI void k1_kstep(K1State& st, int slot) {
  unsigned a0 = st.aK + slot * K1_TILE;
  asm volatile("" : "+v"(a0));
  f32x4 s[4];
#pragma unroll
  for (int kt = 0; kt < 4; ++kt) s[kt] = f32x4{0.f, 0.f, 0.f, 0.f};
#pragma unroll
  for (int sd = 0; sd < 4; ++sd) {
    const unsigned a = a0 ^ (sd << 6);
#pragma unroll
    for (int kt = 0; kt < 4; ++kt) {
      s16x8 kf = lds_b128(a + kt * 4096);
      s[kt] = mfma16(kf, st.q[sd], s[kt]);
    }
  }
  float tm = fmaxf(fmaxf(s[0][0], s[0][1]), fmaxf(s[0][2], s[0][3]));
#pragma unroll
  for (int kt = 1; kt < 4; ++kt) tm = fmaxf(tm, fmaxf(fmaxf(s[kt][0], s[kt][1]), fmaxf(s[kt][2], s[kt][3])));
  tm = xmax16_32(tm);
  const float mn = fmaxf(st.m, tm);
  const float nb = -mn * SC_L2E;
  float ps = 0.f;
#pragma unroll
  for (int kt = 0; kt < 4; ++kt)
#pragma unroll
    for (int r = 0; r < 4; ++r) { float e = __builtin_amdgcn_exp2f(fmaf(s[kt][r], SC_L2E, nb)); s[kt][r] = e; ps += e; }
  if (__any(mn > st.m)) {
    const float al = __builtin_amdgcn_exp2f((st.m - mn) * SC_L2E);
    st.l *= al;
#pragma unroll
    for (int dt = 0; dt < 8; ++dt)
#pragma unroll
      for (int r = 0; r < 4; ++r) st.o[dt][r] *= al;
    st.m = mn;
  }
  st.l += ps;
#pragma unroll
  for (int ks = 0; ks < 2; ++ks) {
    u32x4 w = {cvtpk(s[2 * ks][0], s[2 * ks][1]), cvtpk(s[2 * ks][2], s[2 * ks][3]),
               cvtpk(s[2 * ks + 1][0], s[2 * ks + 1][1]), cvtpk(s[2 * ks + 1][2], s[2 * ks + 1][3])};
    st.p[ks] = __builtin_bit_cast(s16x8, w);
  }
}

DI void k1_vstep(K1State& st, int slot) {
  unsigned a0 = st.aV + slot * K1_TILE;
  asm volatile("" : "+v"(a0));
#pragma unroll
  for (int ks = 0; ks < 2; ++ks) {
#pragma unroll
    for (int dt = 0; dt < 8; ++dt) {
      const unsigned a = (a0 ^ (dt << 5)) + ks * 8192;
      s16x8 vf = lds_tr2(a, a + 4096);
      st.o[dt] = mfma16(vf, st.p[ks], st.o[dt]);
    }
  }
}

template <int NWAIT, bool ISSUE>
DI void k1_pair(K1State& st, int j, int& slot) {
  wait_vm<(NWAIT > 6 ? 6 : NWAIT)>(); bar();
  if (ISSUE && st.wave < 8) { int s2 = slot + 4; if (s2 >= K1_NSLOT) s2 -= K1_NSLOT; k1_issue_kv(st, j + 4, s2); }
  k1_kstep(st, slot);
  slot = (slot + 1 == K1_NSLOT) ? 0 : slot + 1;
  wait_vm<(NWAIT - 2 > 6 ? 6 : NWAIT - 2)>(); bar();
  if (ISSUE && st.wave < 8) { int s2 = slot + 4; if (s2 >= K1_NSLOT) s2 -= K1_NSLOT; k1_issue_kv(st, j + 5, s2); }
  k1_vstep(st, slot);
  slot = (slot + 1 == K1_NSLOT) ? 0 : slot + 1;
}

__global__ __launch_bounds__(K1_WAVES * 64, 5)
void mha_attn_kernel(const __hip_bfloat16* __restrict__ xA, const __hip_bfloat16* __restrict__ kA,
                     const __hip_bfloat16* __restrict__ vA, const __hip_bfloat16* __restrict__ wqA,
                     const __hip_bfloat16* __restrict__ xB, const __hip_bfloat16* __restrict__ kB,
                     const __hip_bfloat16* __restrict__ vB, const __hip_bfloat16* __restrict__ wqB,
                     __hip_bfloat16* __restrict__ ows) {
  const int tid = threadIdx.x;
  const int wave = __builtin_amdgcn_readfirstlane(tid >> 6);
  const int lane = tid & 63;
  const int r16 = lane & 15, g = lane >> 4;
  const int bid = blockIdx.x;
  const int prob = bid >> 8, b = (bid >> 5) & 7, h = bid & 31;

  const char* xg = (const char*)(prob ? xB : xA) + (size_t)(b * SQ + wave * 16) * (DM * 2);
  const char* wg = (const char*)(prob ? wqB : wqA) + (size_t)h * (DH * 2);
  K1State st;
  st.wave = wave;
  st.kg = (const char*)(prob ? kB : kA) + (size_t)(b * NH + h) * (SK * DH * 2);
  st.vg = (const char*)(prob ? vB : vA) + (size_t)(b * NH + h) * (SK * DH * 2);
  st.ldsw = wave * 2048;
  unsigned offW[2];
#pragma unroll
  for (int j = 0; j < 2; ++j) {
    const int row = (wave & 7) * 8 + j * 4 + g;
    const int fK = row & 15;
    const int fV = ((row & 3) | ((((row >> 2) ^ (row >> 3)) & 1) << 2)) << 1;
    st.offK[j] = row * 256 + ((r16 ^ fK) << 4);
    st.offV[j] = row * 256 + ((r16 ^ fV) << 4);
    offW[j]    = row * (HD * 2) + ((r16 ^ fV) << 4);
  }
  st.aK = r16 * 256 + ((g ^ r16) << 4);
  const int qq = r16 >> 2, pp = r16 & 3;
  {
    const int rowl = 4 * g + qq;
    const int f = (rowl & 3) | ((((rowl >> 2) ^ (rowl >> 3)) & 1) << 2);
    st.aV = rowl * 256 + (f << 5) + ((pp >> 1) << 4) + ((pp & 1) << 3);
  }
  unsigned aW;
  {
    const int rowl = 8 * g + qq;
    const int f = (rowl & 3) | ((((rowl >> 2) ^ (rowl >> 3)) & 1) << 2);
    aW = rowl * 256 + (f << 5) + ((pp >> 1) << 4) + ((pp & 1) << 3);
  }
  const unsigned xoff = r16 * (DM * 2) + g * 16;

  s16x8 xf[4][2];
#define X_LOAD(T) do { \
    asm volatile("global_load_dwordx4 %0, %1, %2 offset:%3" : "=v"(xf[(T) & 3][0]) : "v"(xoff), "s"(xg), "i"((T) * 128) : "memory"); \
    asm volatile("global_load_dwordx4 %0, %1, %2 offset:%3" : "=v"(xf[(T) & 3][1]) : "v"(xoff), "s"(xg), "i"((T) * 128 + 64) : "memory"); } while (0)
#define W_ISSUE(T) do { if (wave < 8) { const char* gsrc = wg + (size_t)(T) * 64 * (HD * 2); const unsigned lb = ((T) % K1_NSLOT) * K1_TILE + st.ldsw; \
    dma16(lb, offW[0], gsrc); dma16(lb + 1024, offW[1], gsrc); } } while (0)

#pragma unroll
  for (int t = 0; t < 4; ++t) { W_ISSUE(t); X_LOAD(t); }

  f32x4 qa[8];
#pragma unroll
  for (int dt = 0; dt < 8; ++dt) qa[dt] = f32x4{0.f, 0.f, 0.f, 0.f};

#pragma unroll
  for (int t = 0; t < K1_NW; ++t) {
    constexpr int dummy = 0; (void)dummy;
    const int nx = ((t + 1 <= 9) ? 2 : 0) + ((t + 2 <= 9) ? 2 : 0) + ((t + 3 <= 9) ? 2 : 0);
    if (wave < 8) {
      switch (nx + 6) { case 12: wait_vm<12>(); break; case 10: wait_vm<10>(); break; case 8: wait_vm<8>(); break; default: wait_vm<6>(); break; }
    } else {
      switch (nx) { case 6: wait_vm<6>(); break; case 4: wait_vm<4>(); break; case 2: wait_vm<2>(); break; default: wait_vm<0>(); break; }
    }
    asm volatile("" : "+v"(xf[t & 3][0]), "+v"(xf[t & 3][1]));
    bar();
    if (t + 4 < K1_NW) { W_ISSUE(t + 4); }
    else if (wave < 8) { k1_issue_kv(st, t + 4, (t + 4) % K1_NSLOT); }
    unsigned a0 = aW + (t % K1_NSLOT) * K1_TILE;
    asm volatile("" : "+v"(a0));
#pragma unroll
    for (int kk = 0; kk < 2; ++kk) {
#pragma unroll
      for (int dt = 0; dt < 8; ++dt) {
        const unsigned a = (a0 ^ (dt << 5)) + kk * 8192;
        s16x8 wf = lds_tr2(a, (a ^ 128u) + 1024);
        qa[dt] = mfma16(wf, xf[t & 3][kk], qa[dt]);
      }
    }
    if (t + 4 < K1_NW) { asm volatile("" ::: "memory"); X_LOAD(t + 4); }
  }
  {
    unsigned pk[8][2];
#pragma unroll
    for (int dt = 0; dt < 8; ++dt) { pk[dt][0] = cvtpk(qa[dt][0], qa[dt][1]); pk[dt][1] = cvtpk(qa[dt][2], qa[dt][3]); }
    const int srcA = (r16 + 16 * (2 * (g & 1))) << 2, srcB = srcA + 64;
    const bool hiT = (g >> 1) != 0;
#pragma unroll
    for (int s = 0; s < 4; ++s) {
      u32x4 w;
#pragma unroll
      for (int d = 0; d < 2; ++d) {
        unsigned a0 = __builtin_amdgcn_ds_bpermute(srcA, pk[2 * s][d]), a1 = __builtin_amdgcn_ds_bpermute(srcA, pk[2 * s + 1][d]);
        unsigned b0 = __builtin_amdgcn_ds_bpermute(srcB, pk[2 * s][d]), b1 = __builtin_amdgcn_ds_bpermute(srcB, pk[2 * s + 1][d]);
        w[d] = hiT ? a1 : a0; w[2 + d] = hiT ? b1 : b0;
      }
      st.q[s] = __builtin_bit_cast(s16x8, w);
    }
  }
#pragma unroll
  for (int dt = 0; dt < 8; ++dt) st.o[dt] = f32x4{0.f, 0.f, 0.f, 0.f};
  st.m = -__builtin_inff(); st.l = 0.f;

  int slot = K1_NW % K1_NSLOT;
  int j = K1_NW;
#pragma unroll 1
  for (int it = 0; it < 14; ++it, j += 2) k1_pair<8, true>(st, j, slot);
  k1_pair<6, false>(st, j, slot); j += 2;
  k1_pair<2, false>(st, j, slot);

  const float lt = xsum16_32(st.l);
  const float inv = 1.f / lt;
  char* og = (char*)ows + ((size_t)(prob * MROWS + b * SQ + wave * 16 + r16) * HD + h * DH + 4 * g) * 2;
#pragma unroll
  for (int dt = 0; dt < 8; ++dt) {
    u32x2 w = {cvtpk(st.o[dt][0] * inv, st.o[dt][1] * inv), cvtpk(st.o[dt][2] * inv, st.o[dt][3] * inv)};
    *(u32x2*)(og + dt * 32) = w;
  }
}

constexpr int K2_BM = 96, K2_BN = 64, K2_BK = 128;
constexpr int K2_OT = K2_BM * K2_BK * 2;
constexpr int K2_WT = K2_BK * K2_BN * 2;
constexpr int K2_ST = K2_OT + K2_WT;
constexpr int K2_NS = 3;
constexpr int K2_LDS = K2_NS * K2_ST;
constexpr int K2_STEPS = HD / K2_BK;

__global__ __launch_bounds__(512, 2)
void mha_oproj_kernel(const __hip_bfloat16* __restrict__ ows, const __hip_bfloat16* __restrict__ woA,
                      const __hip_bfloat16* __restrict__ woB, __hip_bfloat16* __restrict__ out) {
  const int tid = threadIdx.x;
  const int wave = __builtin_amdgcn_readfirstlane(tid >> 6);
  const int lane = tid & 63;
  const int r16 = lane & 15, g = lane >> 4;
  const int bid = blockIdx.x;
  const int xcd = bid & 7, loc = bid >> 3;
  const int mblk = xcd * 3 + loc / 10, nblk = loc % 10;
  const int prob = mblk / 12, mb = mblk % 12;
  const char* og = (const char*)ows + (size_t)(prob * MROWS + mb * K2_BM) * (HD * 2);
  const char* wg = (const char*)(prob ? woB : woA) + (size_t)nblk * (K2_BN * 2);
  unsigned offO[3], offWo[2];
#pragma unroll
  for (int j = 0; j < 3; ++j) { const int row = wave * 12 + j * 4 + g; offO[j] = row * (HD * 2) + ((r16 ^ (row & 15)) << 4); }
#pragma unroll
  for (int j = 0; j < 2; ++j) {
    const int row = wave * 16 + j * 8 + (lane >> 3);
    const int f = ((row >> 1) & 1) | (((row >> 3) & 1) << 1);
    offWo[j] = row * (DM * 2) + (((lane & 7) ^ (f << 1)) << 4);
  }
  const int wm = wave >> 2, wn = wave & 3;
  const unsigned aO = (wm * 48 + r16) * 256 + ((g ^ r16) << 4);
  const int qq = r16 >> 2, pp = r16 & 3;
  unsigned aWo;
  {
    const int rowl = 8 * g + qq;
    const int f = ((rowl >> 1) & 1) | (((rowl >> 3) & 1) << 1);
    aWo = K2_OT + rowl * 128 + (((2 * wn + (pp >> 1)) ^ (f << 1)) << 4) + ((pp & 1) << 3);
  }
#define K2_ISSUE(T, SL) do { const char* go = og + (size_t)(T) * (K2_BK * 2); const char* gw = wg + (size_t)(T) * K2_BK * (DM * 2); \
    const unsigned lb = (SL) * K2_ST; \
    dma16(lb + wave * 3072, offO[0], go); dma16(lb + wave * 3072 + 1024, offO[1], go); dma16(lb + wave * 3072 + 2048, offO[2], go); \
    dma16(lb + K2_OT + wave * 2048, offWo[0], gw); dma16(lb + K2_OT + wave * 2048 + 1024, offWo[1], gw); } while (0)
  f32x4 acc[3];
#pragma unroll
  for (int i = 0; i < 3; ++i) acc[i] = f32x4{0.f, 0.f, 0.f, 0.f};
  K2_ISSUE(0, 0); K2_ISSUE(1, 1);
  int slot = 0;
#pragma unroll 1
  for (int t = 0; t < K2_STEPS; ++t) {
    if (t + 1 < K2_STEPS) wait_vm<5>(); else wait_vm<0>();
    bar();
    if (t + 2 < K2_STEPS) { int s2 = slot + 2; if (s2 >= K2_NS) s2 -= K2_NS; K2_ISSUE(t + 2, s2); }
    const unsigned sb = slot * K2_ST;
#pragma unroll
    for (int ks = 0; ks < 4; ++ks) {
      const unsigned aw = aWo + sb + ks * 32 * 128;
      s16x8 wf = lds_tr2(aw, aw + 512);
      const unsigned ao = ((aO) ^ (ks << 6)) + sb;
#pragma unroll
      for (int mt = 0; mt < 3; ++mt) {
        s16x8 of = lds_b128(ao + mt * 4096);
        acc[mt] = mfma16(wf, of, acc[mt]);
      }
    }
    slot = (slot + 1 == K2_NS) ? 0 : slot + 1;
  }
#pragma unroll
  for (int mt = 0; mt < 3; ++mt) {
    const int row = mb * K2_BM + wm * 48 + mt * 16 + r16;
    u32x2 w = {cvtpk(acc[mt][0], acc[mt][1]), cvtpk(acc[mt][2], acc[mt][3])};
    *(u32x2*)((char*)out + ((size_t)row * (2 * DM) + prob * DM + nblk * K2_BN + wn * 16 + 4 * g) * 2) = w;
  }
}

extern "C" void kernel_launch(void* const* d_in, const int* in_sizes, int n_in,
                              void* d_out, int out_size, void* d_ws, size_t ws_size,
                              hipStream_t stream) {
  const __hip_bfloat16* xA  = (const __hip_bfloat16*)d_in[0];
  const __hip_bfloat16* kA  = (const __hip_bfloat16*)d_in[1];
  const __hip_bfloat16* vA  = (const __hip_bfloat16*)d_in[2];
  const __hip_bfloat16* wqA = (const __hip_bfloat16*)d_in[3];
  const __hip_bfloat16* woA = (const __hip_bfloat16*)d_in[4];
  const __hip_bfloat16* xB  = (const __hip_bfloat16*)d_in[5];
  const __hip_bfloat16* kB  = (const __hip_bfloat16*)d_in[6];
  const __hip_bfloat16* vB  = (const __hip_bfloat16*)d_in[7];
  const __hip_bfloat16* wqB = (const __hip_bfloat16*)d_in[8];
  const __hip_bfloat16* woB = (const __hip_bfloat16*)d_in[9];
  __hip_bfloat16* ows = (__hip_bfloat16*)d_ws;
  (void)hipFuncSetAttribute((const void*)mha_attn_kernel, hipFuncAttributeMaxDynamicSharedMemorySize, K1_LDS);
  (void)hipFuncSetAttribute((const void*)mha_oproj_kernel, hipFuncAttributeMaxDynamicSharedMemorySize, K2_LDS);
  hipLaunchKernelGGL(mha_attn_kernel, dim3(512), dim3(K1_WAVES * 64), K1_LDS, stream, xA, kA, vA, wqA, xB, kB, vB, wqB, ows);
  hipLaunchKernelGGL(mha_oproj_kernel, dim3(240), dim3(512), K2_LDS, stream, (const __hip_bfloat16*)ows, woA, woB, (__hip_bfloat16*)d_out);
}
```

```cpp
#include <hip/hip_runtime.h>
#include <hip/hip_bf16.h>
#include <stdint.h>

typedef short    s16x4 __attribute__((ext_vector_type(4)));
typedef short    s16x8 __attribute__((ext_vector_type(8)));
typedef float    f32x4 __attribute__((ext_vector_type(4)));
typedef float    f32x2 __attribute__((ext_vector_type(2)));
typedef __bf16   bf16x2 __attribute__((ext_vector_type(2)));
typedef unsigned u32x4 __attribute__((ext_vector_type(4)));
typedef unsigned u32x2 __attribute__((ext_vector_type(2)));
typedef __attribute__((address_space(3))) s16x4* lds_s16x4_ptr;
typedef __attribute__((address_space(3))) const s16x8* lds_s16x8_ptr;
#define DI __device__ __forceinline__

constexpr int NB = 8, SQ = 144, DM = 640, NH = 32, SK = 1024, DH = 128;
constexpr int HD = NH * DH;
constexpr int MROWS = NB * SQ;

DI unsigned cvtpk(float lo, float hi) {
  f32x2 v = {lo, hi};
  bf16x2 b = __builtin_convertvector(v, bf16x2);
  return __builtin_bit_cast(unsigned, b);
}
DI f32x4 mfma16(s16x8 a, s16x8 b, f32x4 c) {
  return __builtin_amdgcn_mfma_f32_16x16x32_bf16(a, b, c, 0, 0, 0);
}
DI void dma16(unsigned lds_addr, unsigned voff, const char* gbase) {
  asm volatile("s_mov_b32 m0, %0\n\ts_nop 0\n\tglobal_load_lds_dwordx4 %1, %2"
               :: "s"(lds_addr), "v"(voff), "s"(gbase) : "memory");
}
template <int N> DI void wait_vm() { asm volatile("s_waitcnt vmcnt(%0)" :: "i"(N) : "memory"); }
DI void bar() { asm volatile("s_barrier" ::: "memory"); }
DI s16x8 lds_b128(unsigned addr) { return *(lds_s16x8_ptr)(uintptr_t)addr; }
DI s16x8 lds_tr2(unsigned addr0, unsigned addr1) {
  s16x4 lo = __builtin_amdgcn_ds_read_tr16_b64_v4i16((lds_s16x4_ptr)(uintptr_t)addr0);
  s16x4 hi = __builtin_amdgcn_ds_read_tr16_b64_v4i16((lds_s16x4_ptr)(uintptr_t)addr1);
  return __builtin_shufflevector(lo, hi, 0, 1, 2, 3, 4, 5, 6, 7);
}
DI float xmax16_32(float v) {
  auto r = __builtin_amdgcn_permlane32_swap(__float_as_uint(v), __float_as_uint(v), false, false);
  v = fmaxf(__uint_as_float(r[0]), __uint_as_float(r[1]));
  auto r2 = __builtin_amdgcn_permlane16_swap(__float_as_uint(v), __float_as_uint(v), false, false);
  return fmaxf(__uint_as_float(r2[0]), __uint_as_float(r2[1]));
}
DI float xsum16_32(float v) {
  auto r = __builtin_amdgcn_permlane32_swap(__float_as_uint(v), __float_as_uint(v), false, false);
  v = __uint_as_float(r[0]) + __uint_as_float(r[1]);
  auto r2 = __builtin_amdgcn_permlane16_swap(__float_as_uint(v), __float_as_uint(v), false, false);
  return __uint_as_float(r2[0]) + __uint_as_float(r2[1]);
}

constexpr int K1_WAVES = 4;
constexpr int K1_NSLOT = 5;
constexpr int K1_TILE  = 16384;
constexpr int K1_NW    = 10;
constexpr int K1_NT    = K1_NW + 32;
constexpr int K1_LDS   = K1_NSLOT * K1_TILE;
constexpr float SC_L2E = 0.088388347648318447f * 1.4426950408889634f;

DI f32x4 mfma16k16(s16x4 a, s16x4 b, f32x4 c) {
  return __builtin_amdgcn_mfma_f32_16x16x16bf16_1k(a, b, c, 0, 0, 0);
}
DI s16x4 lds_tr(unsigned addr) { return __builtin_amdgcn_ds_read_tr16_b64_v4i16((lds_s16x4_ptr)(uintptr_t)addr); }

struct K1State {
  unsigned offK[4], offV[4];
  const char *kg, *vg;
  unsigned ldsw;
  unsigned aK, aV;
  unsigned w4k;
  s16x8 q[2][4], q8[4];
  f32x4 o[2][8], o8[8];
  float m[2], l[2], m8, l8;
  s16x8 p[2][2];
  s16x4 p8;
};

DI void k1_issue_kv(K1State& st, int j, int slot) {
  const int t = (j - K1_NW) >> 1;
  const unsigned lb = slot * K1_TILE + st.ldsw;
  if ((j - K1_NW) & 1) {
    const char* g = st.vg + (size_t)t * K1_TILE;
#pragma unroll
    for (int i = 0; i < 4; ++i) dma16(lb + i * 1024, st.offV[i], g);
  } else {
    const char* g = st.kg + (size_t)t * K1_TILE;
#pragma unroll
    for (int i = 0; i < 4; ++i) dma16(lb + i * 1024, st.offK[i], g);
  }
}

template <int NT>
DI void k1_softmax(f32x4 (&s)[NT], float& m, float& l, f32x4 (&o)[8]) {
  float tm = fmaxf(fmaxf(s[0][0], s[0][1]), fmaxf(s[0][2], s[0][3]));
#pragma unroll
  for (int kt = 1; kt < NT; ++kt) tm = fmaxf(tm, fmaxf(fmaxf(s[kt][0], s[kt][1]), fmaxf(s[kt][2], s[kt][3])));
  tm = xmax16_32(tm);
  const float mn = fmaxf(m, tm);
  const float nb = -mn * SC_L2E;
  float ps = 0.f;
#pragma unroll
  for (int kt = 0; kt < NT; ++kt)
#pragma unroll
    for (int r = 0; r < 4; ++r) { float e = __builtin_amdgcn_exp2f(fmaf(s[kt][r], SC_L2E, nb)); s[kt][r] = e; ps += e; }
  if (__any(mn > m)) {
    const float al = __builtin_amdgcn_exp2f((m - mn) * SC_L2E);
    l *= al;
#pragma unroll
    for (int dt = 0; dt < 8; ++dt)
#pragma unroll
      for (int r = 0; r < 4; ++r) o[dt][r] *= al;
    m = mn;
  }
  l += ps;
}

DI void k1_kstep(K1State& st, int slot) {
  unsigned a0 = st.aK + slot * K1_TILE;
  asm volatile("" : "+v"(a0));
  f32x4 s0[4], s1[4], s8[1];
#pragma unroll
  for (int kt = 0; kt < 4; ++kt) { s0[kt] = f32x4{0.f, 0.f, 0.f, 0.f}; s1[kt] = f32x4{0.f, 0.f, 0.f, 0.f}; }
  s8[0] = f32x4{0.f, 0.f, 0.f, 0.f};
  s16x8 kf[2][5];
#pragma unroll
  for (int kt = 0; kt < 4; ++kt) kf[0][kt] = lds_b128(a0 + kt * 4096);
  kf[0][4] = lds_b128(a0 + st.w4k);
#pragma unroll
  for (int sd = 0; sd < 4; ++sd) {
    if (sd < 3) {
      const unsigned a = a0 ^ ((sd + 1) << 6);
#pragma unroll
      for (int kt = 0; kt < 4; ++kt) kf[(sd + 1) & 1][kt] = lds_b128(a + kt * 4096);
      kf[(sd + 1) & 1][4] = lds_b128(a + st.w4k);
    }
#pragma unroll
    for (int kt = 0; kt < 4; ++kt) {
      s0[kt] = mfma16(kf[sd & 1][kt], st.q[0][sd], s0[kt]);
      s1[kt] = mfma16(kf[sd & 1][kt], st.q[1][sd], s1[kt]);
    }
    s8[0] = mfma16(kf[sd & 1][4], st.q8[sd], s8[0]);
    __builtin_amdgcn_sched_barrier(0);
  }
  k1_softmax<4>(s0, st.m[0], st.l[0], st.o[0]);
  k1_softmax<4>(s1, st.m[1], st.l[1], st.o[1]);
  k1_softmax<1>(s8, st.m8, st.l8, st.o8);
#pragma unroll
  for (int ks = 0; ks < 2; ++ks) {
    u32x4 w0 = {cvtpk(s0[2 * ks][0], s0[2 * ks][1]), cvtpk(s0[2 * ks][2], s0[2 * ks][3]),
                cvtpk(s0[2 * ks + 1][0], s0[2 * ks + 1][1]), cvtpk(s0[2 * ks + 1][2], s0[2 * ks + 1][3])};
    st.p[0][ks] = __builtin_bit_cast(s16x8, w0);
    u32x4 w1 = {cvtpk(s1[2 * ks][0], s1[2 * ks][1]), cvtpk(s1[2 * ks][2], s1[2 * ks][3]),
                cvtpk(s1[2 * ks + 1][0], s1[2 * ks + 1][1]), cvtpk(s1[2 * ks + 1][2], s1[2 * ks + 1][3])};
    st.p[1][ks] = __builtin_bit_cast(s16x8, w1);
  }
  u32x2 w8 = {cvtpk(s8[0][0], s8[0][1]), cvtpk(s8[0][2], s8[0][3])};
  st.p8 = __builtin_bit_cast(s16x4, w8);
}

DI void k1_vstep(K1State& st, int slot) {
  unsigned a0 = st.aV + slot * K1_TILE;
  asm volatile("" : "+v"(a0));
  s16x8 vf[2][4];
#pragma unroll
  for (int i = 0; i < 4; ++i) { const unsigned a = a0 ^ (i << 5); vf[0][i] = lds_tr2(a, a + 4096); }
#pragma unroll
  for (int sg = 0; sg < 4; ++sg) {
    const int ks = sg >> 1, d0 = (sg & 1) * 4;
    if (sg < 3) {
      const int ks2 = (sg + 1) >> 1, d2 = ((sg + 1) & 1) * 4;
#pragma unroll
      for (int i = 0; i < 4; ++i) { const unsigned a = (a0 ^ ((d2 + i) << 5)) + ks2 * 8192; vf[(sg + 1) & 1][i] = lds_tr2(a, a + 4096); }
    } else {
#pragma unroll
      for (int i = 0; i < 4; ++i) { const unsigned a = (a0 ^ ((2 * i) << 5)) + st.w4k, a2 = (a0 ^ ((2 * i + 1) << 5)) + st.w4k; vf[0][i] = lds_tr2(a, a2); }
    }
#pragma unroll
    for (int i = 0; i < 4; ++i) {
      st.o[0][d0 + i] = mfma16(vf[sg & 1][i], st.p[0][ks], st.o[0][d0 + i]);
      st.o[1][d0 + i] = mfma16(vf[sg & 1][i], st.p[1][ks], st.o[1][d0 + i]);
    }
    __builtin_amdgcn_sched_barrier(0);
  }
#pragma unroll
  for (int i = 0; i < 4; ++i) {
    s16x4 lo = __builtin_shufflevector(vf[0][i], vf[0][i], 0, 1, 2, 3), hi = __builtin_shufflevector(vf[0][i], vf[0][i], 4, 5, 6, 7);
    st.o8[2 * i] = mfma16k16(lo, st.p8, st.o8[2 * i]);
    st.o8[2 * i + 1] = mfma16k16(hi, st.p8, st.o8[2 * i + 1]);
  }
}

template <int NW0, int NW1, bool ISSUE>
DI void k1_pair(K1State& st, int j, int& slot) {
  wait_vm<NW0>(); bar();
  if (ISSUE) { int s2 = slot + 4; if (s2 >= K1_NSLOT) s2 -= K1_NSLOT; k1_issue_kv(st, j + 4, s2); }
  k1_kstep(st, slot);
  slot = (slot + 1 == K1_NSLOT) ? 0 : slot + 1;
  wait_vm<NW1>(); bar();
  if (ISSUE) { int s2 = slot + 4; if (s2 >= K1_NSLOT) s2 -= K1_NSLOT; k1_issue_kv(st, j + 5, s2); }
  k1_vstep(st, slot);
  slot = (slot + 1 == K1_NSLOT) ? 0 : slot + 1;
}

DI s16x8 k1_qfrag(const f32x4& t0, const f32x4& t1, int srcA, int srcB, bool hiT) {
  unsigned p00 = cvtpk(t0[0], t0[1]), p01 = cvtpk(t0[2], t0[3]), p10 = cvtpk(t1[0], t1[1]), p11 = cvtpk(t1[2], t1[3]);
  u32x4 w;
  { unsigned a0 = __builtin_amdgcn_ds_bpermute(srcA, p00), a1 = __builtin_amdgcn_ds_bpermute(srcA, p10); w[0] = hiT ? a1 : a0; }
  { unsigned a0 = __builtin_amdgcn_ds_bpermute(srcA, p01), a1 = __builtin_amdgcn_ds_bpermute(srcA, p11); w[1] = hiT ? a1 : a0; }
  { unsigned a0 = __builtin_amdgcn_ds_bpermute(srcB, p00), a1 = __builtin_amdgcn_ds_bpermute(srcB, p10); w[2] = hiT ? a1 : a0; }
  { unsigned a0 = __builtin_amdgcn_ds_bpermute(srcB, p01), a1 = __builtin_amdgcn_ds_bpermute(srcB, p11); w[3] = hiT ? a1 : a0; }
  return __builtin_bit_cast(s16x8, w);
}

__global__ __launch_bounds__(K1_WAVES * 64, 2)
void mha_attn_kernel(const __hip_bfloat16* __restrict__ xA, const __hip_bfloat16* __restrict__ kA,
                     const __hip_bfloat16* __restrict__ vA, const __hip_bfloat16* __restrict__ wqA,
                     const __hip_bfloat16* __restrict__ xB, const __hip_bfloat16* __restrict__ kB,
                     const __hip_bfloat16* __restrict__ vB, const __hip_bfloat16* __restrict__ wqB,
                     __hip_bfloat16* __restrict__ ows) {
  const int tid = threadIdx.x;
  const int wave = __builtin_amdgcn_readfirstlane(tid >> 6);
  const int lane = tid & 63;
  const int r16 = lane & 15, g = lane >> 4;
  const int bid = blockIdx.x;
  const int prob = bid >> 8, b = (bid >> 5) & 7, h = bid & 31;

  const char* xg = (const char*)(prob ? xB : xA) + (size_t)(b * SQ) * (DM * 2);
  const char* wg = (const char*)(prob ? wqB : wqA) + (size_t)h * (DH * 2);
  K1State st;
  st.kg = (const char*)(prob ? kB : kA) + (size_t)(b * NH + h) * (SK * DH * 2);
  st.vg = (const char*)(prob ? vB : vA) + (size_t)(b * NH + h) * (SK * DH * 2);
  st.ldsw = wave * 4096;
  st.w4k = wave * 4096;
  unsigned offW[4];
#pragma unroll
  for (int j = 0; j < 4; ++j) {
    const int row = wave * 16 + j * 4 + g;
    const int fK = row & 15;
    const int fV = ((row & 3) | ((((row >> 2) ^ (row >> 3)) & 1) << 2)) << 1;
    st.offK[j] = row * 256 + ((r16 ^ fK) << 4);
    st.offV[j] = row * 256 + ((r16 ^ fV) << 4);
    offW[j]    = row * (HD * 2) + ((r16 ^ fV) << 4);
  }
  st.aK = r16 * 256 + ((g ^ r16) << 4);
  const int qq = r16 >> 2, pp = r16 & 3;
  {
    const int rowl = 4 * g + qq;
    const int f = (rowl & 3) | ((((rowl >> 2) ^ (rowl >> 3)) & 1) << 2);
    st.aV = rowl * 256 + (f << 5) + ((pp >> 1) << 4) + ((pp & 1) << 3);
  }
  unsigned aW;
  {
    const int rowl = 8 * g + qq;
    const int f = (rowl & 3) | ((((rowl >> 2) ^ (rowl >> 3)) & 1) << 2);
    aW = rowl * 256 + (f << 5) + ((pp >> 1) << 4) + ((pp & 1) << 3);
  }
  const unsigned xoff0 = (wave * 32 + r16) * (DM * 2) + g * 16;
  const unsigned xoff1 = xoff0 + 16 * (DM * 2);
  const unsigned xoff8 = (128 + r16) * (DM * 2) + g * 16;

  s16x8 xf[4][3][2];
#define X_LOAD1(T, I, OFF) do { \
    asm volatile("global_load_dwordx4 %0, %1, %2 offset:%3" : "=v"(xf[(T) & 3][I][0]) : "v"(OFF), "s"(xg), "i"((T) * 128) : "memory"); \
    asm volatile("global_load_dwordx4 %0, %1, %2 offset:%3" : "=v"(xf[(T) & 3][I][1]) : "v"(OFF), "s"(xg), "i"((T) * 128 + 64) : "memory"); } while (0)
#define X_LOAD(T) do { X_LOAD1(T, 0, xoff0); X_LOAD1(T, 1, xoff1); X_LOAD1(T, 2, xoff8); } while (0)
#define W_ISSUE(T) do { const char* gsrc = wg + (size_t)(T) * 64 * (HD * 2); const unsigned lb = ((T) % K1_NSLOT) * K1_TILE + st.ldsw; \
    dma16(lb, offW[0], gsrc); dma16(lb + 1024, offW[1], gsrc); dma16(lb + 2048, offW[2], gsrc); dma16(lb + 3072, offW[3], gsrc); } while (0)

#pragma unroll
  for (int t = 0; t < 4; ++t) { W_ISSUE(t); X_LOAD(t); }

  f32x4 qa0[8], qa1[8], qa8[2];
#pragma unroll
  for (int dt = 0; dt < 8; ++dt) { qa0[dt] = f32x4{0.f, 0.f, 0.f, 0.f}; qa1[dt] = f32x4{0.f, 0.f, 0.f, 0.f}; }
  qa8[0] = f32x4{0.f, 0.f, 0.f, 0.f}; qa8[1] = f32x4{0.f, 0.f, 0.f, 0.f};
  const unsigned w8x = (unsigned)wave << 6;

#pragma unroll
  for (int t = 0; t < K1_NW; ++t) {
    const int nx = ((t + 1 <= 9) ? 6 : 0) + ((t + 2 <= 9) ? 6 : 0) + ((t + 3 <= 9) ? 6 : 0);
    switch (nx + 12) { case 30: wait_vm<30>(); break; case 24: wait_vm<24>(); break; case 18: wait_vm<18>(); break; default: wait_vm<12>(); break; }
    asm volatile("" : "+v"(xf[t & 3][0][0]), "+v"(xf[t & 3][0][1]), "+v"(xf[t & 3][1][0]), "+v"(xf[t & 3][1][1]), "+v"(xf[t & 3][2][0]), "+v"(xf[t & 3][2][1]));
    bar();
    if (t + 4 < K1_NW) { W_ISSUE(t + 4); }
    else { k1_issue_kv(st, t + 4, (t + 4) % K1_NSLOT); }
    unsigned a0 = aW + (t % K1_NSLOT) * K1_TILE;
    asm volatile("" : "+v"(a0));
    {
      s16x8 wf[2][4];
#pragma unroll
      for (int i = 0; i < 4; ++i) { const unsigned a = a0 ^ (i << 5); wf[0][i] = lds_tr2(a, (a ^ 128u) + 1024); }
#pragma unroll
      for (int sg = 0; sg < 4; ++sg) {
        const int kk = sg >> 1, d0 = (sg & 1) * 4;
        if (sg < 3) {
          const int kk2 = (sg + 1) >> 1, d2 = ((sg + 1) & 1) * 4;
#pragma unroll
          for (int i = 0; i < 4; ++i) { const unsigned a = (a0 ^ ((d2 + i) << 5)) + kk2 * 8192; wf[(sg + 1) & 1][i] = lds_tr2(a, (a ^ 128u) + 1024); }
        } else {
#pragma unroll
          for (int i = 0; i < 4; ++i) { const unsigned a = (a0 ^ w8x ^ ((i & 1) << 5)) + (i >> 1) * 8192; wf[0][i] = lds_tr2(a, (a ^ 128u) + 1024); }
        }
#pragma unroll
        for (int i = 0; i < 4; ++i) {
          qa0[d0 + i] = mfma16(wf[sg & 1][i], xf[t & 3][0][kk], qa0[d0 + i]);
          qa1[d0 + i] = mfma16(wf[sg & 1][i], xf[t & 3][1][kk], qa1[d0 + i]);
        }
        __builtin_amdgcn_sched_barrier(0);
      }
#pragma unroll
      for (int i = 0; i < 4; ++i) qa8[i & 1] = mfma16(wf[0][i], xf[t & 3][2][i >> 1], qa8[i & 1]);
    }
    if (t + 4 < K1_NW) { asm volatile("" ::: "memory"); X_LOAD(t + 4); }
  }
  {
    const int srcA = (r16 + 16 * (2 * (g & 1))) << 2, srcB = srcA + 64;
    const bool hiT = (g >> 1) != 0;
#pragma unroll
    for (int s = 0; s < 4; ++s) {
      st.q[0][s] = k1_qfrag(qa0[2 * s], qa0[2 * s + 1], srcA, srcB, hiT);
      st.q[1][s] = k1_qfrag(qa1[2 * s], qa1[2 * s + 1], srcA, srcB, hiT);
    }
    s16x8 q8w = k1_qfrag(qa8[0], qa8[1], srcA, srcB, hiT);
    const unsigned sc = ((K1_NW - 1) % K1_NSLOT) * K1_TILE;
    asm volatile("s_waitcnt lgkmcnt(0)" ::: "memory"); bar();
    *(__attribute__((address_space(3))) s16x8*)(uintptr_t)(sc + wave * 1024 + lane * 16) = q8w;
    asm volatile("s_waitcnt lgkmcnt(0)" ::: "memory"); bar();
#pragma unroll
    for (int s = 0; s < 4; ++s) st.q8[s] = lds_b128(sc + s * 1024 + lane * 16);
    asm volatile("s_waitcnt lgkmcnt(0)" ::: "memory");
  }
#pragma unroll
  for (int dt = 0; dt < 8; ++dt) { st.o[0][dt] = f32x4{0.f, 0.f, 0.f, 0.f}; st.o[1][dt] = f32x4{0.f, 0.f, 0.f, 0.f}; st.o8[dt] = f32x4{0.f, 0.f, 0.f, 0.f}; }
  st.m[0] = st.m[1] = st.m8 = -__builtin_inff(); st.l[0] = st.l[1] = st.l8 = 0.f;

  int slot = K1_NW % K1_NSLOT;
  int j = K1_NW;
#pragma unroll 1
  for (int it = 0; it < 14; ++it, j += 2) k1_pair<12, 12, true>(st, j, slot);
  k1_pair<12, 8, false>(st, j, slot); j += 2;
  k1_pair<4, 0, false>(st, j, slot);

  int tz = threadIdx.x; asm volatile("" : "+v"(tz));
  char* ogb = (char*)ows + ((size_t)(prob * MROWS + b * SQ + (tz & 15)) * HD + h * DH + 4 * ((tz >> 4) & 3)) * 2;
#pragma unroll
  for (int i = 0; i < 2; ++i) {
    const float inv = 1.f / xsum16_32(st.l[i]);
    char* og = ogb + (size_t)(wave * 32 + i * 16) * (HD * 2);
#pragma unroll
    for (int dt = 0; dt < 8; ++dt) {
      u32x2 w = {cvtpk(st.o[i][dt][0] * inv, st.o[i][dt][1] * inv), cvtpk(st.o[i][dt][2] * inv, st.o[i][dt][3] * inv)};
      *(u32x2*)(og + dt * 32) = w;
    }
  }
  {
    typedef __attribute__((address_space(3))) f32x4* lds_f32x4_ptr;
    typedef __attribute__((address_space(3))) float* lds_f32_ptr;
    const float l8t = xsum16_32(st.l8);
    asm volatile("s_waitcnt lgkmcnt(0)" ::: "memory"); bar();
#pragma unroll
    for (int dt = 0; dt < 8; ++dt) *(lds_f32x4_ptr)(uintptr_t)(wave * 8192 + dt * 1024 + lane * 16) = st.o8[dt];
    if (g == 0) { *(lds_f32_ptr)(uintptr_t)(32768 + (wave * 16 + r16) * 4) = st.m8; *(lds_f32_ptr)(uintptr_t)(32768 + 256 + (wave * 16 + r16) * 4) = l8t; }
    asm volatile("s_waitcnt lgkmcnt(0)" ::: "memory"); bar();
    float mw[4], lw[4];
#pragma unroll
    for (int w = 0; w < 4; ++w) { mw[w] = *(lds_f32_ptr)(uintptr_t)(32768 + (w * 16 + r16) * 4); lw[w] = *(lds_f32_ptr)(uintptr_t)(32768 + 256 + (w * 16 + r16) * 4); }
    const float M = fmaxf(fmaxf(mw[0], mw[1]), fmaxf(mw[2], mw[3]));
    float L = 0.f, sc[4];
#pragma unroll
    for (int w = 0; w < 4; ++w) { sc[w] = __builtin_amdgcn_exp2f((mw[w] - M) * SC_L2E); L += lw[w] * sc[w]; }
    const float inv = 1.f / L;
    char* og = ogb + (size_t)128 * (HD * 2);
#pragma unroll
    for (int d2 = 0; d2 < 2; ++d2) {
      const int dt = 2 * wave + d2;
      f32x4 acc = {0.f, 0.f, 0.f, 0.f};
#pragma unroll
      for (int w = 0; w < 4; ++w) { f32x4 v = *(lds_f32x4_ptr)(uintptr_t)(w * 8192 + dt * 1024 + lane * 16); acc += v * sc[w]; }
      u32x2 wv = {cvtpk(acc[0] * inv, acc[1] * inv), cvtpk(acc[2] * inv, acc[3] * inv)};
      *(u32x2*)(og + dt * 32) = wv;
    }
  }
}

constexpr int K2_BM = 96, K2_BN = 64, K2_BK = 128;
constexpr int K2_OT = K2_BM * K2_BK * 2;
constexpr int K2_WT = K2_BK * K2_BN * 2;
constexpr int K2_ST = K2_OT + K2_WT;
constexpr int K2_NS = 3;
constexpr int K2_LDS = K2_NS * K2_ST;
constexpr int K2_STEPS = HD / K2_BK;

__global__ __launch_bounds__(512, 2)
void mha_oproj_kernel(const __hip_bfloat16* __restrict__ ows, const __hip_bfloat16* __restrict__ woA,
                      const __hip_bfloat16* __restrict__ woB, __hip_bfloat16* __restrict__ out) {
  const int tid = threadIdx.x;
  const int wave = __builtin_amdgcn_readfirstlane(tid >> 6);
  const int lane = tid & 63;
  const int r16 = lane & 15, g = lane >> 4;
  const int bid = blockIdx.x;
  const int xcd = bid & 7, loc = bid >> 3;
  const int mblk = xcd * 3 + loc / 10, nblk = loc % 10;
  const int prob = mblk / 12, mb = mblk % 12;
  const char* og = (const char*)ows + (size_t)(prob * MROWS + mb * K2_BM) * (HD * 2);
  const char* wg = (const char*)(prob ? woB : woA) + (size_t)nblk * (K2_BN * 2);
  unsigned offO[3], offWo[2];
#pragma unroll
  for (int j = 0; j < 3; ++j) { const int row = wave * 12 + j * 4 + g; offO[j] = row * (HD * 2) + ((r16 ^ (row & 15)) << 4); }
#pragma unroll
  for (int j = 0; j < 2; ++j) {
    const int row = wave * 16 + j * 8 + (lane >> 3);
    const int f = ((row >> 1) & 1) | (((row >> 3) & 1) << 1);
    offWo[j] = row * (DM * 2) + (((lane & 7) ^ (f << 1)) << 4);
  }
  const int wm = wave >> 2, wn = wave & 3;
  const unsigned aO = (wm * 48 + r16) * 256 + ((g ^ r16) << 4);
  const int qq = r16 >> 2, pp = r16 & 3;
  unsigned aWo;
  {
    const int rowl = 8 * g + qq;
    const int f = ((rowl >> 1) & 1) | (((rowl >> 3) & 1) << 1);
    aWo = K2_OT + rowl * 128 + (((2 * wn + (pp >> 1)) ^ (f << 1)) << 4) + ((pp & 1) << 3);
  }
#define K2_ISSUE(T, SL) do { const char* go = og + (size_t)(T) * (K2_BK * 2); const char* gw = wg + (size_t)(T) * K2_BK * (DM * 2); \
    const unsigned lb = (SL) * K2_ST; \
    dma16(lb + wave * 3072, offO[0], go); dma16(lb + wave * 3072 + 1024, offO[1], go); dma16(lb + wave * 3072 + 2048, offO[2], go); \
    dma16(lb + K2_OT + wave * 2048, offWo[0], gw); dma16(lb + K2_OT + wave * 2048 + 1024, offWo[1], gw); } while (0)
  f32x4 acc[3];
#pragma unroll
  for (int i = 0; i < 3; ++i) acc[i] = f32x4{0.f, 0.f, 0.f, 0.f};
  K2_ISSUE(0, 0); K2_ISSUE(1, 1);
  int slot = 0;
#pragma unroll 1
  for (int t = 0; t < K2_STEPS; ++t) {
    if (t + 1 < K2_STEPS) wait_vm<5>(); else wait_vm<0>();
    bar();
    if (t + 2 < K2_STEPS) { int s2 = slot + 2; if (s2 >= K2_NS) s2 -= K2_NS; K2_ISSUE(t + 2, s2); }
    const unsigned sb = slot * K2_ST;
#pragma unroll
    for (int ks = 0; ks < 4; ++ks) {
      const unsigned aw = aWo + sb + ks * 32 * 128;
      s16x8 wf = lds_tr2(aw, aw + 512);
      const unsigned ao = ((aO) ^ (ks << 6)) + sb;
#pragma unroll
      for (int mt = 0; mt < 3; ++mt) {
        s16x8 of = lds_b128(ao + mt * 4096);
        acc[mt] = mfma16(wf, of, acc[mt]);
      }
    }
    slot = (slot + 1 == K2_NS) ? 0 : slot + 1;
  }
#pragma unroll
  for (int mt = 0; mt < 3; ++mt) {
    const int row = mb * K2_BM + wm * 48 + mt * 16 + r16;
    u32x2 w = {cvtpk(acc[mt][0], acc[mt][1]), cvtpk(acc[mt][2], acc[mt][3])};
    *(u32x2*)((char*)out + ((size_t)row * (2 * DM) + prob * DM + nblk * K2_BN + wn * 16 + 4 * g) * 2) = w;
  }
}

extern "C" void kernel_launch(void* const* d_in, const int* in_sizes, int n_in,
                              void* d_out, int out_size, void* d_ws, size_t ws_size,
                              hipStream_t stream) {
  const __hip_bfloat16* xA  = (const __hip_bfloat16*)d_in[0];
  const __hip_bfloat16* kA  = (const __hip_bfloat16*)d_in[1];
  const __hip_bfloat16* vA  = (const __hip_bfloat16*)d_in[2];
  const __hip_bfloat16* wqA = (const __hip_bfloat16*)d_in[3];
  const __hip_bfloat16* woA = (const __hip_bfloat16*)d_in[4];
  const __hip_bfloat16* xB  = (const __hip_bfloat16*)d_in[5];
  const __hip_bfloat16* kB  = (const __hip_bfloat16*)d_in[6];
  const __hip_bfloat16* vB  = (const __hip_bfloat16*)d_in[7];
  const __hip_bfloat16* wqB = (const __hip_bfloat16*)d_in[8];
  const __hip_bfloat16* woB = (const __hip_bfloat16*)d_in[9];
  __hip_bfloat16* ows = (__hip_bfloat16*)d_ws;
  (void)hipFuncSetAttribute((const void*)mha_attn_kernel, hipFuncAttributeMaxDynamicSharedMemorySize, K1_LDS);
  (void)hipFuncSetAttribute((const void*)mha_oproj_kernel, hipFuncAttributeMaxDynamicSharedMemorySize, K2_LDS);
  hipLaunchKernelGGL(mha_attn_kernel, dim3(512), dim3(K1_WAVES * 64), K1_LDS, stream, xA, kA, vA, wqA, xB, kB, vB, wqB, ows);
  hipLaunchKernelGGL(mha_oproj_kernel, dim3(240), dim3(512), K2_LDS, stream, (const __hip_bfloat16*)ows, woA, woB, (__hip_bfloat16*)d_out);
}
```

```cpp
#include <hip/hip_runtime.h>
#include <hip/hip_bf16.h>
#include <stdint.h>

typedef short    s16x4 __attribute__((ext_vector_type(4)));
typedef short    s16x8 __attribute__((ext_vector_type(8)));
typedef float    f32x4 __attribute__((ext_vector_type(4)));
typedef float    f32x2 __attribute__((ext_vector_type(2)));
typedef __bf16   bf16x2 __attribute__((ext_vector_type(2)));
typedef unsigned u32x4 __attribute__((ext_vector_type(4)));
typedef unsigned u32x2 __attribute__((ext_vector_type(2)));
typedef __attribute__((address_space(3))) s16x4* lds_s16x4_ptr;
typedef __attribute__((address_space(3))) const s16x8* lds_s16x8_ptr;
#define DI __device__ __forceinline__

constexpr int NB = 8, SQ = 144, DM = 640, NH = 32, SK = 1024, DH = 128;
constexpr int HD = NH * DH;
constexpr int MROWS = NB * SQ;

DI unsigned cvtpk(float lo, float hi) {
  f32x2 v = {lo, hi};
  bf16x2 b = __builtin_convertvector(v, bf16x2);
  return __builtin_bit_cast(unsigned, b);
}
DI f32x4 mfma16(s16x8 a, s16x8 b, f32x4 c) {
  return __builtin_amdgcn_mfma_f32_16x16x32_bf16(a, b, c, 0, 0, 0);
}
DI void dma16(unsigned lds_addr, unsigned voff, const char* gbase) {
  asm volatile("s_mov_b32 m0, %0\n\ts_nop 0\n\tglobal_load_lds_dwordx4 %1, %2"
               :: "s"(lds_addr), "v"(voff), "s"(gbase) : "memory");
}
DI void dma16nt(unsigned lds_addr, unsigned voff, const char* gbase) {
  asm volatile("s_mov_b32 m0, %0\n\ts_nop 0\n\tglobal_load_lds_dwordx4 %1, %2 nt"
               :: "s"(lds_addr), "v"(voff), "s"(gbase) : "memory");
}
template <int N> DI void wait_vm() { asm volatile("s_waitcnt vmcnt(%0)" :: "i"(N) : "memory"); }
DI void bar() { asm volatile("s_barrier" ::: "memory"); }
DI s16x8 lds_b128(unsigned addr) { return *(lds_s16x8_ptr)(uintptr_t)addr; }
DI s16x8 lds_tr2(unsigned addr0, unsigned addr1) {
  s16x4 lo = __builtin_amdgcn_ds_read_tr16_b64_v4i16((lds_s16x4_ptr)(uintptr_t)addr0);
  s16x4 hi = __builtin_amdgcn_ds_read_tr16_b64_v4i16((lds_s16x4_ptr)(uintptr_t)addr1);
  return __builtin_shufflevector(lo, hi, 0, 1, 2, 3, 4, 5, 6, 7);
}
DI float xmax16_32(float v) {
  auto r = __builtin_amdgcn_permlane32_swap(__float_as_uint(v), __float_as_uint(v), false, false);
  v = fmaxf(__uint_as_float(r[0]), __uint_as_float(r[1]));
  auto r2 = __builtin_amdgcn_permlane16_swap(__float_as_uint(v), __float_as_uint(v), false, false);
  return fmaxf(__uint_as_float(r2[0]), __uint_as_float(r2[1]));
}
DI float xsum16_32(float v) {
  auto r = __builtin_amdgcn_permlane32_swap(__float_as_uint(v), __float_as_uint(v), false, false);
  v = __uint_as_float(r[0]) + __uint_as_float(r[1]);
  auto r2 = __builtin_amdgcn_permlane16_swap(__float_as_uint(v), __float_as_uint(v), false, false);
  return __uint_as_float(r2[0]) + __uint_as_float(r2[1]);
}

typedef float f32x16 __attribute__((ext_vector_type(16)));
constexpr int K1_WAVES = 4;
constexpr int K1_NSLOT = 5;
constexpr int K1_TILE  = 16384;
constexpr int K1_NW    = 10;
constexpr int K1_NT    = K1_NW + 32;
constexpr int K1_LDS   = K1_NSLOT * K1_TILE;
constexpr float SC_L2E = 0.088388347648318447f * 1.4426950408889634f;
constexpr float K1_THR = 8.0f;

DI f32x4 mfma16k16(s16x4 a, s16x4 b, f32x4 c) { return __builtin_amdgcn_mfma_f32_16x16x16bf16_1k(a, b, c, 0, 0, 0); }
DI f32x16 mfma32(s16x8 a, s16x8 b, f32x16 c) { return __builtin_amdgcn_mfma_f32_32x32x16_bf16(a, b, c, 0, 0, 0); }
DI s16x4 lds_tr(unsigned addr) { return __builtin_amdgcn_ds_read_tr16_b64_v4i16((lds_s16x4_ptr)(uintptr_t)addr); }
DI float max3f(float a, float b, float c) { float r; asm("v_max3_f32 %0, %1, %2, %3" : "=v"(r) : "v"(a), "v"(b), "v"(c)); return r; }
DI float max2f(float a, float b) { float r; asm("v_max_f32_e32 %0, %1, %2" : "=v"(r) : "v"(a), "v"(b)); return r; }
DI float xmax32(float v) {
  auto r = __builtin_amdgcn_permlane32_swap(__float_as_uint(v), __float_as_uint(v), false, false);
  return max2f(__uint_as_float(r[0]), __uint_as_float(r[1]));
}
DI float xmax16(float v) {
  auto r = __builtin_amdgcn_permlane16_swap(__float_as_uint(v), __float_as_uint(v), false, false);
  return max2f(__uint_as_float(r[0]), __uint_as_float(r[1]));
}
DI float xsum32(float v) {
  auto r = __builtin_amdgcn_permlane32_swap(__float_as_uint(v), __float_as_uint(v), false, false);
  return __uint_as_float(r[0]) + __uint_as_float(r[1]);
}
DI u32x4 pack_swap(float a0, float a1, float a2, float a3, float b0, float b1, float b2, float b3) {
  unsigned A0 = cvtpk(a0, a1), A1 = cvtpk(a2, a3), B0 = cvtpk(b0, b1), B1 = cvtpk(b2, b3);
  auto r0 = __builtin_amdgcn_permlane32_swap(A0, B0, false, false);
  auto r1 = __builtin_amdgcn_permlane32_swap(A1, B1, false, false);
  return u32x4{r0[0], r1[0], r0[1], r1[1]};
}

struct K1State {
  unsigned offK[4], offV[4];
  const char *kg, *vg;
  unsigned ldsw;
  unsigned aK, aV, aK8, aV8;
  int rot;
  s16x8 q[8], q8[4];
  f32x16 o[4]; f32x4 o8[8];
  f32x16 negm; f32x4 negm8;
  float m, l, m8, l8;
  s16x8 p[4];
  s16x4 p8;
};

DI void k1_issue_kv(K1State& st, int j, int slot) {
  const int t = (((j - K1_NW) >> 1) + st.rot) & 15;
  const unsigned lb = slot * K1_TILE + st.ldsw;
  if ((j - K1_NW) & 1) {
    const char* g = st.vg + (size_t)t * K1_TILE;
#pragma unroll
    for (int i = 0; i < 4; ++i) dma16nt(lb + i * 1024, st.offV[i], g);
  } else {
    const char* g = st.kg + (size_t)t * K1_TILE;
#pragma unroll
    for (int i = 0; i < 4; ++i) dma16nt(lb + i * 1024, st.offK[i], g);
  }
}

DI void k1_kstep(K1State& st, int slot, bool FIRST) {
  unsigned a0 = st.aK + slot * K1_TILE, a8 = st.aK8 + slot * K1_TILE;
  asm volatile("" : "+v"(a0), "+v"(a8));
  f32x16 s[2]; f32x4 s8;
  s16x8 kf[3][2], k8[2];
#pragma unroll
  for (int i = 0; i < 2; ++i) { kf[i][0] = lds_b128(a0 ^ (i << 5)); kf[i][1] = lds_b128((a0 ^ (i << 5)) + 8192); }
  k8[0] = lds_b128(a8);
#pragma unroll
  for (int sg = 0; sg < 8; ++sg) {
    if (sg < 6) { kf[(sg + 2) % 3][0] = lds_b128(a0 ^ ((sg + 2) << 5)); kf[(sg + 2) % 3][1] = lds_b128((a0 ^ ((sg + 2) << 5)) + 8192); }
    if ((sg & 1) == 0 && sg < 6) k8[((sg >> 1) + 1) & 1] = lds_b128(a8 ^ (((sg >> 1) + 1) << 6));
    if (sg == 0) {
      s[0] = mfma32(kf[0][0], st.q[0], st.negm);
      s[1] = mfma32(kf[0][1], st.q[0], st.negm);
      s8 = mfma16(k8[0], st.q8[0], st.negm8);
    } else {
      s[0] = mfma32(kf[sg % 3][0], st.q[sg], s[0]);
      s[1] = mfma32(kf[sg % 3][1], st.q[sg], s[1]);
      if ((sg & 1) == 0) s8 = mfma16(k8[(sg >> 1) & 1], st.q8[sg >> 1], s8);
    }
    __builtin_amdgcn_sched_barrier(0);
  }
  float t0 = max3f(s[0][0], s[0][1], s[0][2]), t1 = max3f(s[1][0], s[1][1], s[1][2]);
#pragma unroll
  for (int r = 3; r < 15; r += 2) { t0 = max3f(t0, s[0][r], s[0][r + 1]); t1 = max3f(t1, s[1][r], s[1][r + 1]); }
  float tm = max3f(t0, t1, max2f(s[0][15], s[1][15]));
  tm = xmax32(tm);
  float t8 = max2f(max2f(s8[0], s8[1]), max2f(s8[2], s8[3]));
  t8 = xmax16(xmax32(t8));
  if (FIRST || __any((tm > K1_THR) | (t8 > K1_THR))) {
    const float dl = FIRST ? tm : max2f(tm, 0.f);
    const float d8 = FIRST ? t8 : max2f(t8, 0.f);
    st.m += dl; st.m8 += d8;
#pragma unroll
    for (int r = 0; r < 16; ++r) { s[0][r] -= dl; s[1][r] -= dl; st.negm[r] = -st.m; }
#pragma unroll
    for (int r = 0; r < 4; ++r) { s8[r] -= d8; st.negm8[r] = -st.m8; }
    {
      const float f = FIRST ? 0.f : __builtin_amdgcn_exp2f(-dl), f8 = FIRST ? 0.f : __builtin_amdgcn_exp2f(-d8);
      st.l *= f; st.l8 *= f8;
#pragma unroll
      for (int dt = 0; dt < 4; ++dt)
#pragma unroll
        for (int r = 0; r < 16; ++r) st.o[dt][r] *= f;
#pragma unroll
      for (int dt = 0; dt < 8; ++dt)
#pragma unroll
        for (int r = 0; r < 4; ++r) st.o8[dt][r] *= f8;
    }
  }
  float ps0 = 0.f, ps1 = 0.f, ps8 = 0.f;
#pragma unroll
  for (int r = 0; r < 16; ++r) {
    float e0 = __builtin_amdgcn_exp2f(s[0][r]), e1 = __builtin_amdgcn_exp2f(s[1][r]);
    s[0][r] = e0; s[1][r] = e1; ps0 += e0; ps1 += e1;
  }
#pragma unroll
  for (int r = 0; r < 4; ++r) { float e = __builtin_amdgcn_exp2f(s8[r]); s8[r] = e; ps8 += e; }
  st.l += ps0 + ps1; st.l8 += ps8;
#pragma unroll
  for (int ks = 0; ks < 4; ++ks) {
    const int kt = ks >> 1, b = 8 * (ks & 1);
    u32x4 w = {cvtpk(s[kt][b], s[kt][b + 1]), cvtpk(s[kt][b + 2], s[kt][b + 3]), cvtpk(s[kt][b + 4], s[kt][b + 5]), cvtpk(s[kt][b + 6], s[kt][b + 7])};
    st.p[ks] = __builtin_bit_cast(s16x8, w);
  }
  u32x2 w8 = {cvtpk(s8[0], s8[1]), cvtpk(s8[2], s8[3])};
  st.p8 = __builtin_bit_cast(s16x4, w8);
}

DI void k1_vstep(K1State& st, int slot) {
  unsigned a0 = st.aV + slot * K1_TILE, a8 = st.aV8 + slot * K1_TILE;
  asm volatile("" : "+v"(a0), "+v"(a8));
  s16x8 vf[2][4];
#pragma unroll
  for (int i = 0; i < 4; ++i) { const unsigned a = a0 ^ (i << 6); vf[0][i] = lds_tr2(a, (a ^ 32u) + 2048); }
#pragma unroll
  for (int ks = 0; ks < 4; ++ks) {
    if (ks < 3) {
#pragma unroll
      for (int i = 0; i < 4; ++i) { const unsigned a = (a0 ^ (i << 6)) + (ks + 1) * 4096; vf[(ks + 1) & 1][i] = lds_tr2(a, (a ^ 32u) + 2048); }
    } else {
#pragma unroll
      for (int i = 0; i < 4; ++i) vf[0][i] = lds_tr2(a8 ^ ((2 * i) << 5), a8 ^ ((2 * i + 1) << 5));
    }
#pragma unroll
    for (int i = 0; i < 4; ++i) st.o[i] = mfma32(vf[ks & 1][i], st.p[ks], st.o[i]);
    __builtin_amdgcn_sched_barrier(0);
  }
#pragma unroll
  for (int i = 0; i < 4; ++i) {
    s16x4 lo = __builtin_shufflevector(vf[0][i], vf[0][i], 0, 1, 2, 3), hi = __builtin_shufflevector(vf[0][i], vf[0][i], 4, 5, 6, 7);
    st.o8[2 * i] = mfma16k16(lo, st.p8, st.o8[2 * i]);
    st.o8[2 * i + 1] = mfma16k16(hi, st.p8, st.o8[2 * i + 1]);
  }
}

DI void k1_pair(K1State& st, int it, int& slot) {
  const int j = K1_NW + 2 * it;
  if (it < 15) wait_vm<12>(); else wait_vm<4>();
  bar();
  if (it < 14) { int s2 = slot + 4; if (s2 >= K1_NSLOT) s2 -= K1_NSLOT; k1_issue_kv(st, j + 4, s2); }
  k1_kstep(st, slot, it == 0);
  slot = (slot + 1 == K1_NSLOT) ? 0 : slot + 1;
  if (it < 14) wait_vm<12>(); else if (it == 14) wait_vm<8>(); else wait_vm<0>();
  bar();
  if (it < 14) { int s2 = slot + 4; if (s2 >= K1_NSLOT) s2 -= K1_NSLOT; k1_issue_kv(st, j + 5, s2); }
  k1_vstep(st, slot);
  slot = (slot + 1 == K1_NSLOT) ? 0 : slot + 1;
}

DI s16x8 k1_qfrag16(const f32x4& t0, const f32x4& t1, int srcA, int srcB, bool hiT) {
  unsigned p00 = cvtpk(t0[0], t0[1]), p01 = cvtpk(t0[2], t0[3]), p10 = cvtpk(t1[0], t1[1]), p11 = cvtpk(t1[2], t1[3]);
  u32x4 w;
  { unsigned a0 = __builtin_amdgcn_ds_bpermute(srcA, p00), a1 = __builtin_amdgcn_ds_bpermute(srcA, p10); w[0] = hiT ? a1 : a0; }
  { unsigned a0 = __builtin_amdgcn_ds_bpermute(srcA, p01), a1 = __builtin_amdgcn_ds_bpermute(srcA, p11); w[1] = hiT ? a1 : a0; }
  { unsigned a0 = __builtin_amdgcn_ds_bpermute(srcB, p00), a1 = __builtin_amdgcn_ds_bpermute(srcB, p10); w[2] = hiT ? a1 : a0; }
  { unsigned a0 = __builtin_amdgcn_ds_bpermute(srcB, p01), a1 = __builtin_amdgcn_ds_bpermute(srcB, p11); w[3] = hiT ? a1 : a0; }
  return __builtin_bit_cast(s16x8, w);
}

__global__ __launch_bounds__(K1_WAVES * 64, 2)
void mha_attn_kernel(const __hip_bfloat16* __restrict__ xA, const __hip_bfloat16* __restrict__ kA,
                     const __hip_bfloat16* __restrict__ vA, const __hip_bfloat16* __restrict__ wqA,
                     const __hip_bfloat16* __restrict__ xB, const __hip_bfloat16* __restrict__ kB,
                     const __hip_bfloat16* __restrict__ vB, const __hip_bfloat16* __restrict__ wqB,
                     __hip_bfloat16* __restrict__ ows) {
  const int tid = threadIdx.x;
  const int wave = __builtin_amdgcn_readfirstlane(tid >> 6);
  const int lane = tid & 63;
  const int r16 = lane & 15, g = lane >> 4, r32 = lane & 31, hh = lane >> 5, blk = g & 1;
  const int bid = blockIdx.x;
  const int xcd = bid & 7, loc = bid >> 3;
  const int prob = xcd & 1, h = ((xcd >> 1) & 1) * 16 + (loc & 15), b = (xcd >> 2) * 4 + (loc >> 4);

  const char* xg = (const char*)(prob ? xB : xA) + (size_t)(b * SQ) * (DM * 2);
  const char* wg = (const char*)(prob ? wqB : wqA) + (size_t)h * (DH * 2);
  K1State st;
  st.kg = (const char*)(prob ? kB : kA) + (size_t)(b * NH + h) * (SK * DH * 2);
  st.vg = (const char*)(prob ? vB : vA) + (size_t)(b * NH + h) * (SK * DH * 2);
  st.ldsw = wave * 4096;
  st.rot = (h + 5 * b) & 15;
  unsigned offW[4];
#pragma unroll
  for (int j = 0; j < 4; ++j) {
    const int row = wave * 16 + j * 4 + g;
    const int fK = row & 15;
    const int fV = ((row & 3) << 2) | ((((row >> 2) ^ (row >> 3)) & 1) << 1);
    st.offK[j] = row * 256 + ((r16 ^ fK) << 4);
    st.offV[j] = row * 256 + ((r16 ^ fV) << 4);
    offW[j]    = row * (HD * 2) + ((r16 ^ fV) << 4);
  }
  const int qq = r16 >> 2, pp = r16 & 3;
  const unsigned pb = ((pp >> 1) << 4) + ((pp & 1) << 3);
  st.aK  = r32 * 256 + ((hh ^ (r32 & 15)) << 4);
  st.aK8 = (wave * 16 + r16) * 256 + ((g ^ r16) << 4);
  st.aV  = (4 * hh + qq) * 256 + (qq << 6) + ((blk ^ hh) << 5) + pb;
  st.aV8 = (wave * 16 + 4 * g + qq) * 256 + (qq << 6) + ((((g & 1) ^ (g >> 1))) << 5) + pb;
  const unsigned aW  = (8 * hh + qq) * 256 + (qq << 6) + ((blk ^ hh) << 5) + pb;
  const unsigned aW8 = ((8 * g + qq) * 256 + (qq << 6) + ((g & 1) << 5) + pb) ^ ((unsigned)wave << 6);
  const unsigned xoff  = (wave * 32 + r32) * (DM * 2) + hh * 16;
  const unsigned xoff8 = (128 + r16) * (DM * 2) + g * 16;

  s16x8 xf[4][4], xf8[4][2];
#define X_LD(DST, OFF, IMM) asm volatile("global_load_dwordx4 %0, %1, %2 offset:%3" : "=v"(DST) : "v"(OFF), "s"(xg), "i"(IMM) : "memory")
#define X_LOAD(T) do { X_LD(xf[(T) & 3][0], xoff, (T) * 128); X_LD(xf[(T) & 3][1], xoff, (T) * 128 + 32); X_LD(xf[(T) & 3][2], xoff, (T) * 128 + 64); X_LD(xf[(T) & 3][3], xoff, (T) * 128 + 96); \
    X_LD(xf8[(T) & 3][0], xoff8, (T) * 128); X_LD(xf8[(T) & 3][1], xoff8, (T) * 128 + 64); } while (0)
#define W_ISSUE(T) do { const char* gsrc = wg + (size_t)(T) * 64 * (HD * 2); const unsigned lb = ((T) % K1_NSLOT) * K1_TILE + st.ldsw; \
    dma16(lb, offW[0], gsrc); dma16(lb + 1024, offW[1], gsrc); dma16(lb + 2048, offW[2], gsrc); dma16(lb + 3072, offW[3], gsrc); } while (0)

#pragma unroll
  for (int t = 0; t < 4; ++t) { W_ISSUE(t); X_LOAD(t); }

  f32x16 qa[4]; f32x4 qa8[2];
#pragma unroll
  for (int dt = 0; dt < 4; ++dt)
#pragma unroll
    for (int r = 0; r < 16; ++r) qa[dt][r] = 0.f;
  qa8[0] = f32x4{0.f, 0.f, 0.f, 0.f}; qa8[1] = f32x4{0.f, 0.f, 0.f, 0.f};

#pragma unroll
  for (int t = 0; t < K1_NW; ++t) {
    const int nx = ((t + 1 <= 9) ? 6 : 0) + ((t + 2 <= 9) ? 6 : 0) + ((t + 3 <= 9) ? 6 : 0);
    switch (nx + 12) { case 30: wait_vm<30>(); break; case 24: wait_vm<24>(); break; case 18: wait_vm<18>(); break; default: wait_vm<12>(); break; }
    asm volatile("" : "+v"(xf[t & 3][0]), "+v"(xf[t & 3][1]), "+v"(xf[t & 3][2]), "+v"(xf[t & 3][3]), "+v"(xf8[t & 3][0]), "+v"(xf8[t & 3][1]));
    bar();
    if (t + 4 < K1_NW) { W_ISSUE(t + 4); }
    else { k1_issue_kv(st, t + 4, (t + 4) % K1_NSLOT); }
    unsigned a0 = aW + (t % K1_NSLOT) * K1_TILE, a8 = aW8 + (t % K1_NSLOT) * K1_TILE;
    asm volatile("" : "+v"(a0), "+v"(a8));
    {
      s16x8 wf[2][4];
#pragma unroll
      for (int i = 0; i < 4; ++i) { const unsigned a = a0 ^ (i << 6); wf[0][i] = lds_tr2(a, (a ^ 32u) + 1024); }
#pragma unroll
      for (int kk = 0; kk < 4; ++kk) {
        if (kk < 3) {
#pragma unroll
          for (int i = 0; i < 4; ++i) { const unsigned a = (a0 ^ (i << 6)) + (kk + 1) * 4096; wf[(kk + 1) & 1][i] = lds_tr2(a, (a ^ 32u) + 1024); }
        } else {
#pragma unroll
          for (int i = 0; i < 4; ++i) { const unsigned a = (a8 ^ ((i & 1) << 5)) + (i >> 1) * 8192; wf[0][i] = lds_tr2(a, (a ^ 32u) + 1024); }
        }
#pragma unroll
        for (int i = 0; i < 4; ++i) qa[i] = mfma32(wf[kk & 1][i], xf[t & 3][kk], qa[i]);
        __builtin_amdgcn_sched_barrier(0);
      }
#pragma unroll
      for (int i = 0; i < 4; ++i) qa8[i & 1] = mfma16(wf[0][i], xf8[t & 3][i >> 1], qa8[i & 1]);
    }
    if (t + 4 < K1_NW) { asm volatile("" ::: "memory"); X_LOAD(t + 4); }
  }
  {
#pragma unroll
    for (int s = 0; s < 8; ++s) {
      const f32x16& a = qa[s >> 1]; const int b0 = 8 * (s & 1);
      u32x4 w = pack_swap(a[b0] * SC_L2E, a[b0 + 1] * SC_L2E, a[b0 + 2] * SC_L2E, a[b0 + 3] * SC_L2E,
                          a[b0 + 4] * SC_L2E, a[b0 + 5] * SC_L2E, a[b0 + 6] * SC_L2E, a[b0 + 7] * SC_L2E);
      st.q[s] = __builtin_bit_cast(s16x8, w);
    }
    const int srcA = (r16 + 16 * (2 * (g & 1))) << 2, srcB = srcA + 64;
    const bool hiT = (g >> 1) != 0;
    s16x8 q8w = k1_qfrag16(qa8[0] * SC_L2E, qa8[1] * SC_L2E, srcA, srcB, hiT);
    const unsigned sc = ((K1_NW - 1) % K1_NSLOT) * K1_TILE;
    asm volatile("s_waitcnt lgkmcnt(0)" ::: "memory"); bar();
    *(__attribute__((address_space(3))) s16x8*)(uintptr_t)(sc + wave * 1024 + lane * 16) = q8w;
    asm volatile("s_waitcnt lgkmcnt(0)" ::: "memory"); bar();
#pragma unroll
    for (int s = 0; s < 4; ++s) st.q8[s] = lds_b128(sc + s * 1024 + lane * 16);
    asm volatile("s_waitcnt lgkmcnt(0)" ::: "memory");
  }
#pragma unroll
  for (int dt = 0; dt < 4; ++dt)
#pragma unroll
    for (int r = 0; r < 16; ++r) st.o[dt][r] = 0.f;
#pragma unroll
  for (int dt = 0; dt < 8; ++dt) st.o8[dt] = f32x4{0.f, 0.f, 0.f, 0.f};
#pragma unroll
  for (int r = 0; r < 16; ++r) st.negm[r] = 0.f;
  st.negm8 = f32x4{0.f, 0.f, 0.f, 0.f};
  st.m = st.m8 = 0.f; st.l = st.l8 = 0.f;

  int slot = K1_NW % K1_NSLOT;
#pragma unroll 1
  for (int it = 0; it < 16; ++it) k1_pair(st, it, slot);

  int tz = threadIdx.x; asm volatile("" : "+v"(tz));
  char* ob = (char*)ows + ((size_t)(prob * MROWS + b * SQ) * HD + h * DH) * 2;
  {
    const float inv = 1.f / xsum32(st.l);
    char* og = ob + (size_t)(wave * 32 + (tz & 31)) * (HD * 2) + ((tz >> 5) & 1) * 16;
#pragma unroll
    for (int dt = 0; dt < 4; ++dt)
#pragma unroll
      for (int s2 = 0; s2 < 2; ++s2) {
        const f32x16& a = st.o[dt]; const int b0 = 8 * s2;
        u32x4 w = pack_swap(a[b0] * inv, a[b0 + 1] * inv, a[b0 + 2] * inv, a[b0 + 3] * inv, a[b0 + 4] * inv, a[b0 + 5] * inv, a[b0 + 6] * inv, a[b0 + 7] * inv);
        *(u32x4*)(og + dt * 64 + s2 * 32) = w;
      }
  }
  {
    typedef __attribute__((address_space(3))) f32x4* lds_f32x4_ptr;
    typedef __attribute__((address_space(3))) float* lds_f32_ptr;
    const int l2 = tz & 63, r = tz & 15;
    const float l8t = xsum16_32(st.l8);
    asm volatile("s_waitcnt lgkmcnt(0)" ::: "memory"); bar();
#pragma unroll
    for (int dt = 0; dt < 8; ++dt) *(lds_f32x4_ptr)(uintptr_t)(wave * 8192 + dt * 1024 + l2 * 16) = st.o8[dt];
    if (l2 < 16) { *(lds_f32_ptr)(uintptr_t)(32768 + (wave * 16 + r) * 4) = st.m8; *(lds_f32_ptr)(uintptr_t)(32768 + 256 + (wave * 16 + r) * 4) = l8t; }
    asm volatile("s_waitcnt lgkmcnt(0)" ::: "memory"); bar();
    float mw[4], lw[4];
#pragma unroll
    for (int w = 0; w < 4; ++w) { mw[w] = *(lds_f32_ptr)(uintptr_t)(32768 + (w * 16 + r) * 4); lw[w] = *(lds_f32_ptr)(uintptr_t)(32768 + 256 + (w * 16 + r) * 4); }
    const float M = fmaxf(fmaxf(mw[0], mw[1]), fmaxf(mw[2], mw[3]));
    float L = 0.f, sc[4];
#pragma unroll
    for (int w = 0; w < 4; ++w) { sc[w] = __builtin_amdgcn_exp2f(mw[w] - M); L += lw[w] * sc[w]; }
    const float inv = 1.f / L;
    char* og = ob + (size_t)(128 + r) * (HD * 2) + ((l2 >> 4) & 3) * 8;
#pragma unroll
    for (int d2 = 0; d2 < 2; ++d2) {
      const int dt = 2 * wave + d2;
      f32x4 acc = {0.f, 0.f, 0.f, 0.f};
#pragma unroll
      for (int w = 0; w < 4; ++w) { f32x4 v = *(lds_f32x4_ptr)(uintptr_t)(w * 8192 + dt * 1024 + l2 * 16); acc += v * sc[w]; }
      u32x2 wv = {cvtpk(acc[0] * inv, acc[1] * inv), cvtpk(acc[2] * inv, acc[3] * inv)};
      *(u32x2*)(og + dt * 32) = wv;
    }
  }
}

constexpr int K2_BM = 96, K2_BN = 64, K2_BK = 128;
constexpr int K2_OT = K2_BM * K2_BK * 2;
constexpr int K2_WT = K2_BK * K2_BN * 2;
constexpr int K2_ST = K2_OT + K2_WT;
constexpr int K2_NS = 3;
constexpr int K2_LDS = K2_NS * K2_ST;
constexpr int K2_STEPS = HD / K2_BK;

__global__ __launch_bounds__(512, 2)
void mha_oproj_kernel(const __hip_bfloat16* __restrict__ ows, const __hip_bfloat16* __restrict__ woA,
                      const __hip_bfloat16* __restrict__ woB, __hip_bfloat16* __restrict__ out) {
  const int tid = threadIdx.x;
  const int wave = __builtin_amdgcn_readfirstlane(tid >> 6);
  const int lane = tid & 63;
  const int r16 = lane & 15, g = lane >> 4;
  const int bid = blockIdx.x;
  const int xcd = bid & 7, loc = bid >> 3;
  const int mblk = xcd * 3 + loc / 10, nblk = loc % 10;
  const int prob = mblk / 12, mb = mblk % 12;
  const char* og = (const char*)ows + (size_t)(prob * MROWS + mb * K2_BM) * (HD * 2);
  const char* wg = (const char*)(prob ? woB : woA) + (size_t)nblk * (K2_BN * 2);
  unsigned offO[3], offWo[2];
#pragma unroll
  for (int j = 0; j < 3; ++j) { const int row = wave * 12 + j * 4 + g; offO[j] = row * (HD * 2) + ((r16 ^ (row & 15)) << 4); }
#pragma unroll
  for (int j = 0; j < 2; ++j) {
    const int row = wave * 16 + j * 8 + (lane >> 3);
    const int f = ((row >> 1) & 1) | (((row >> 3) & 1) << 1);
    offWo[j] = row * (DM * 2) + (((lane & 7) ^ (f << 1)) << 4);
  }
  const int wm = wave >> 2, wn = wave & 3;
  const unsigned aO = (wm * 48 + r16) * 256 + ((g ^ r16) << 4);
  const int qq = r16 >> 2, pp = r16 & 3;
  unsigned aWo;
  {
    const int rowl = 8 * g + qq;
    const int f = ((rowl >> 1) & 1) | (((rowl >> 3) & 1) << 1);
    aWo = K2_OT + rowl * 128 + (((2 * wn + (pp >> 1)) ^ (f << 1)) << 4) + ((pp & 1) << 3);
  }
#define K2_ISSUE(T, SL) do { const char* go = og + (size_t)(T) * (K2_BK * 2); const char* gw = wg + (size_t)(T) * K2_BK * (DM * 2); \
    const unsigned lb = (SL) * K2_ST; \
    dma16(lb + wave * 3072, offO[0], go); dma16(lb + wave * 3072 + 1024, offO[1], go); dma16(lb + wave * 3072 + 2048, offO[2], go); \
    dma16(lb + K2_OT + wave * 2048, offWo[0], gw); dma16(lb + K2_OT + wave * 2048 + 1024, offWo[1], gw); } while (0)
  f32x4 acc[3];
#pragma unroll
  for (int i = 0; i < 3; ++i) acc[i] = f32x4{0.f, 0.f, 0.f, 0.f};
  K2_ISSUE(0, 0); K2_ISSUE(1, 1);
  int slot = 0;
#pragma unroll 1
  for (int t = 0; t < K2_STEPS; ++t) {
    if (t + 1 < K2_STEPS) wait_vm<5>(); else wait_vm<0>();
    bar();
    if (t + 2 < K2_STEPS) { int s2 = slot + 2; if (s2 >= K2_NS) s2 -= K2_NS; K2_ISSUE(t + 2, s2); }
    const unsigned sb = slot * K2_ST;
#pragma unroll
    for (int ks = 0; ks < 4; ++ks) {
      const unsigned aw = aWo + sb + ks * 32 * 128;
      s16x8 wf = lds_tr2(aw, aw + 512);
      const unsigned ao = ((aO) ^ (ks << 6)) + sb;
#pragma unroll
      for (int mt = 0; mt < 3; ++mt) {
        s16x8 of = lds_b128(ao + mt * 4096);
        acc[mt] = mfma16(wf, of, acc[mt]);
      }
    }
    slot = (slot + 1 == K2_NS) ? 0 : slot + 1;
  }
#pragma unroll
  for (int mt = 0; mt < 3; ++mt) {
    const int row = mb * K2_BM + wm * 48 + mt * 16 + r16;
    u32x2 w = {cvtpk(acc[mt][0], acc[mt][1]), cvtpk(acc[mt][2], acc[mt][3])};
    *(u32x2*)((char*)out + ((size_t)row * (2 * DM) + prob * DM + nblk * K2_BN + wn * 16 + 4 * g) * 2) = w;
  }
}

extern "C" void kernel_launch(void* const* d_in, const int* in_sizes, int n_in,
                              void* d_out, int out_size, void* d_ws, size_t ws_size,
                              hipStream_t stream) {
  const __hip_bfloat16* xA  = (const __hip_bfloat16*)d_in[0];
  const __hip_bfloat16* kA  = (const __hip_bfloat16*)d_in[1];
  const __hip_bfloat16* vA  = (const __hip_bfloat16*)d_in[2];
  const __hip_bfloat16* wqA = (const __hip_bfloat16*)d_in[3];
  const __hip_bfloat16* woA = (const __hip_bfloat16*)d_in[4];
  const __hip_bfloat16* xB  = (const __hip_bfloat16*)d_in[5];
  const __hip_bfloat16* kB  = (const __hip_bfloat16*)d_in[6];
  const __hip_bfloat16* vB  = (const __hip_bfloat16*)d_in[7];
  const __hip_bfloat16* wqB = (const __hip_bfloat16*)d_in[8];
  const __hip_bfloat16* woB = (const __hip_bfloat16*)d_in[9];
  __hip_bfloat16* ows = (__hip_bfloat16*)d_ws;
  (void)hipFuncSetAttribute((const void*)mha_attn_kernel, hipFuncAttributeMaxDynamicSharedMemorySize, K1_LDS);
  (void)hipFuncSetAttribute((const void*)mha_oproj_kernel, hipFuncAttributeMaxDynamicSharedMemorySize, K2_LDS);
  hipLaunchKernelGGL(mha_attn_kernel, dim3(512), dim3(K1_WAVES * 64), K1_LDS, stream, xA, kA, vA, wqA, xB, kB, vB, wqB, ows);
  hipLaunchKernelGGL(mha_oproj_kernel, dim3(240), dim3(512), K2_LDS, stream, (const __hip_bfloat16*)ows, woA, woB, (__hip_bfloat16*)d_out);
}
```

```cpp
#include <hip/hip_runtime.h>
#include <hip/hip_bf16.h>
#include <stdint.h>

typedef short    s16x4 __attribute__((ext_vector_type(4)));
typedef short    s16x8 __attribute__((ext_vector_type(8)));
typedef float    f32x4 __attribute__((ext_vector_type(4)));
typedef float    f32x2 __attribute__((ext_vector_type(2)));
typedef __bf16   bf16x2 __attribute__((ext_vector_type(2)));
typedef unsigned u32x4 __attribute__((ext_vector_type(4)));
typedef unsigned u32x2 __attribute__((ext_vector_type(2)));
typedef __attribute__((address_space(3))) s16x4* lds_s16x4_ptr;
typedef __attribute__((address_space(3))) const s16x8* lds_s16x8_ptr;
#define DI __device__ __forceinline__

constexpr int NB = 8, SQ = 144, DM = 640, NH = 32, SK = 1024, DH = 128;
constexpr int HD = NH * DH;
constexpr int MROWS = NB * SQ;

DI unsigned cvtpk(float lo, float hi) {
  f32x2 v = {lo, hi};
  bf16x2 b = __builtin_convertvector(v, bf16x2);
  return __builtin_bit_cast(unsigned, b);
}
DI f32x4 mfma16(s16x8 a, s16x8 b, f32x4 c) {
  return __builtin_amdgcn_mfma_f32_16x16x32_bf16(a, b, c, 0, 0, 0);
}
DI void dma16(unsigned lds_addr, unsigned voff, const char* gbase) {
  asm volatile("s_mov_b32 m0, %0\n\ts_nop 0\n\tglobal_load_lds_dwordx4 %1, %2"
               :: "s"(lds_addr), "v"(voff), "s"(gbase) : "memory");
}
DI void dma16nt(unsigned lds_addr, unsigned voff, const char* gbase) {
  asm volatile("s_mov_b32 m0, %0\n\ts_nop 0\n\tglobal_load_lds_dwordx4 %1, %2 nt"
               :: "s"(lds_addr), "v"(voff), "s"(gbase) : "memory");
}
template <int N> DI void wait_vm() { asm volatile("s_waitcnt vmcnt(%0)" :: "i"(N) : "memory"); }
DI void bar() { asm volatile("s_waitcnt lgkmcnt(0)\n\ts_barrier" ::: "memory"); }
DI s16x8 lds_b128(unsigned addr) { return *(lds_s16x8_ptr)(uintptr_t)addr; }
DI s16x8 lds_tr2(unsigned addr0, unsigned addr1) {
  s16x4 lo = __builtin_amdgcn_ds_read_tr16_b64_v4i16((lds_s16x4_ptr)(uintptr_t)addr0);
  s16x4 hi = __builtin_amdgcn_ds_read_tr16_b64_v4i16((lds_s16x4_ptr)(uintptr_t)addr1);
  return __builtin_shufflevector(lo, hi, 0, 1, 2, 3, 4, 5, 6, 7);
}
DI float xmax16_32(float v) {
  auto r = __builtin_amdgcn_permlane32_swap(__float_as_uint(v), __float_as_uint(v), false, false);
  v = fmaxf(__uint_as_float(r[0]), __uint_as_float(r[1]));
  auto r2 = __builtin_amdgcn_permlane16_swap(__float_as_uint(v), __float_as_uint(v), false, false);
  return fmaxf(__uint_as_float(r2[0]), __uint_as_float(r2[1]));
}
DI float xsum16_32(float v) {
  auto r = __builtin_amdgcn_permlane32_swap(__float_as_uint(v), __float_as_uint(v), false, false);
  v = __uint_as_float(r[0]) + __uint_as_float(r[1]);
  auto r2 = __builtin_amdgcn_permlane16_swap(__float_as_uint(v), __float_as_uint(v), false, false);
  return __uint_as_float(r2[0]) + __uint_as_float(r2[1]);
}

typedef float f32x16 __attribute__((ext_vector_type(16)));
constexpr int K1_WAVES = 4;
constexpr int K1_NSLOT = 5;
constexpr int K1_TILE  = 16384;
constexpr int K1_NWS   = 10;
constexpr int K1_NW    = 20;
constexpr int K1_LDS   = K1_NSLOT * K1_TILE;
constexpr float SC_L2E = 0.088388347648318447f * 1.4426950408889634f;
constexpr float K1_THR = 8.0f;

DI f32x4 mfma16k16(s16x4 a, s16x4 b, f32x4 c) { return __builtin_amdgcn_mfma_f32_16x16x16bf16_1k(a, b, c, 0, 0, 0); }
DI f32x16 mfma32(s16x8 a, s16x8 b, f32x16 c) { return __builtin_amdgcn_mfma_f32_32x32x16_bf16(a, b, c, 0, 0, 0); }
DI s16x4 lds_tr(unsigned addr) { return __builtin_amdgcn_ds_read_tr16_b64_v4i16((lds_s16x4_ptr)(uintptr_t)addr); }
DI float max3f(float a, float b, float c) { float r; asm("v_max3_f32 %0, %1, %2, %3" : "=v"(r) : "v"(a), "v"(b), "v"(c)); return r; }
DI float max2f(float a, float b) { float r; asm("v_max_f32_e32 %0, %1, %2" : "=v"(r) : "v"(a), "v"(b)); return r; }
DI float xmax32(float v) {
  auto r = __builtin_amdgcn_permlane32_swap(__float_as_uint(v), __float_as_uint(v), false, false);
  return max2f(__uint_as_float(r[0]), __uint_as_float(r[1]));
}
DI float xmax16(float v) {
  auto r = __builtin_amdgcn_permlane16_swap(__float_as_uint(v), __float_as_uint(v), false, false);
  return max2f(__uint_as_float(r[0]), __uint_as_float(r[1]));
}
DI float xsum32(float v) {
  auto r = __builtin_amdgcn_permlane32_swap(__float_as_uint(v), __float_as_uint(v), false, false);
  return __uint_as_float(r[0]) + __uint_as_float(r[1]);
}
DI u32x4 pack_swap(float a0, float a1, float a2, float a3, float b0, float b1, float b2, float b3) {
  unsigned A0 = cvtpk(a0, a1), A1 = cvtpk(a2, a3), B0 = cvtpk(b0, b1), B1 = cvtpk(b2, b3);
  auto r0 = __builtin_amdgcn_permlane32_swap(A0, B0, false, false);
  auto r1 = __builtin_amdgcn_permlane32_swap(A1, B1, false, false);
  return u32x4{r0[0], r1[0], r0[1], r1[1]};
}

#define STAMP(i) do {} while (0)
struct K1State {
  unsigned offK[4], offV[4];
  const char *kg, *vg;
  unsigned ldsw;
  unsigned aK, aV, aK8, aV8;
  int rot;
  s16x8 q[8], q8[4];
  f32x16 o[4]; f32x4 o8[8];
  f32x16 negm; f32x4 negm8;
  float m, l, m8, l8;
  s16x8 p[4];
  s16x4 p8;
};

DI void k1_issue_kv(K1State& st, int j, int slot) {
  const int t = (((j - K1_NW) >> 1) + st.rot) & 15;
  const unsigned lb = slot * K1_TILE + st.ldsw;
  if ((j - K1_NW) & 1) {
    const char* g = st.vg + (size_t)t * K1_TILE;
#pragma unroll
    for (int i = 0; i < 4; ++i) dma16nt(lb + i * 1024, st.offV[i], g);
  } else {
    const char* g = st.kg + (size_t)t * K1_TILE;
#pragma unroll
    for (int i = 0; i < 4; ++i) dma16nt(lb + i * 1024, st.offK[i], g);
  }
}

DI void k1_issue_piece(K1State& st, int j, int slot, int i) {
  const int t = (((j - K1_NW) >> 1) + st.rot) & 15;
  const unsigned lb = slot * K1_TILE + st.ldsw + i * 1024;
  if ((j - K1_NW) & 1) dma16nt(lb, st.offV[i], st.vg + (size_t)t * K1_TILE);
  else                 dma16nt(lb, st.offK[i], st.kg + (size_t)t * K1_TILE);
}

DI void k1_kstep(K1State& st, int slot, bool FIRST, bool ISSUE, int jn, int sn) {
  unsigned a0 = st.aK + slot * K1_TILE, a8 = st.aK8 + slot * K1_TILE;
  asm volatile("" : "+v"(a0), "+v"(a8));
  f32x16 s[2]; f32x4 s8;
  s16x8 kf[3][2], k8[2];
#pragma unroll
  for (int i = 0; i < 2; ++i) { kf[i][0] = lds_b128(a0 ^ (i << 5)); kf[i][1] = lds_b128((a0 ^ (i << 5)) + 8192); }
  k8[0] = lds_b128(a8);
#pragma unroll
  for (int sg = 0; sg < 8; ++sg) {
    if (sg < 6) { kf[(sg + 2) % 3][0] = lds_b128(a0 ^ ((sg + 2) << 5)); kf[(sg + 2) % 3][1] = lds_b128((a0 ^ ((sg + 2) << 5)) + 8192); }
    if ((sg & 1) == 0 && sg < 6) k8[((sg >> 1) + 1) & 1] = lds_b128(a8 ^ (((sg >> 1) + 1) << 6));
    if (sg == 0) {
      s[0] = mfma32(kf[0][0], st.q[0], st.negm);
      s[1] = mfma32(kf[0][1], st.q[0], st.negm);
      s8 = mfma16(k8[0], st.q8[0], st.negm8);
    } else {
      s[0] = mfma32(kf[sg % 3][0], st.q[sg], s[0]);
      s[1] = mfma32(kf[sg % 3][1], st.q[sg], s[1]);
      if ((sg & 1) == 0) s8 = mfma16(k8[(sg >> 1) & 1], st.q8[sg >> 1], s8);
    }
    __builtin_amdgcn_sched_barrier(0);
    if ((sg & 1) == 1 && ISSUE) k1_issue_piece(st, jn, sn, sg >> 1);
    __builtin_amdgcn_sched_barrier(0);
  }
  STAMP(1);
  float t0 = max3f(s[0][0], s[0][1], s[0][2]), t1 = max3f(s[1][0], s[1][1], s[1][2]);
#pragma unroll
  for (int r = 3; r < 15; r += 2) { t0 = max3f(t0, s[0][r], s[0][r + 1]); t1 = max3f(t1, s[1][r], s[1][r + 1]); }
  float tm = max3f(t0, t1, max2f(s[0][15], s[1][15]));
  tm = xmax32(tm);
  float t8 = max2f(max2f(s8[0], s8[1]), max2f(s8[2], s8[3]));
  t8 = xmax16(xmax32(t8));
  if (FIRST || __any((tm > K1_THR) | (t8 > K1_THR))) {
    const float dl = FIRST ? tm : max2f(tm, 0.f);
    const float d8 = FIRST ? t8 : max2f(t8, 0.f);
    st.m += dl; st.m8 += d8;
#pragma unroll
    for (int r = 0; r < 16; ++r) { s[0][r] -= dl; s[1][r] -= dl; st.negm[r] = -st.m; }
#pragma unroll
    for (int r = 0; r < 4; ++r) { s8[r] -= d8; st.negm8[r] = -st.m8; }
    {
      const float f = FIRST ? 0.f : __builtin_amdgcn_exp2f(-dl), f8 = FIRST ? 0.f : __builtin_amdgcn_exp2f(-d8);
      st.l *= f; st.l8 *= f8;
#pragma unroll
      for (int dt = 0; dt < 4; ++dt)
#pragma unroll
        for (int r = 0; r < 16; ++r) st.o[dt][r] *= f;
#pragma unroll
      for (int dt = 0; dt < 8; ++dt)
#pragma unroll
        for (int r = 0; r < 4; ++r) st.o8[dt][r] *= f8;
    }
  }
  float ps0 = 0.f, ps1 = 0.f, ps8 = 0.f;
#pragma unroll
  for (int r = 0; r < 16; ++r) {
    float e0 = __builtin_amdgcn_exp2f(s[0][r]), e1 = __builtin_amdgcn_exp2f(s[1][r]);
    s[0][r] = e0; s[1][r] = e1; ps0 += e0; ps1 += e1;
  }
#pragma unroll
  for (int r = 0; r < 4; ++r) { float e = __builtin_amdgcn_exp2f(s8[r]); s8[r] = e; ps8 += e; }
  st.l += ps0 + ps1; st.l8 += ps8;
#pragma unroll
  for (int ks = 0; ks < 4; ++ks) {
    const int kt = ks >> 1, b = 8 * (ks & 1);
    u32x4 w = {cvtpk(s[kt][b], s[kt][b + 1]), cvtpk(s[kt][b + 2], s[kt][b + 3]), cvtpk(s[kt][b + 4], s[kt][b + 5]), cvtpk(s[kt][b + 6], s[kt][b + 7])};
    st.p[ks] = __builtin_bit_cast(s16x8, w);
  }
  u32x2 w8 = {cvtpk(s8[0], s8[1]), cvtpk(s8[2], s8[3])};
  st.p8 = __builtin_bit_cast(s16x4, w8);
}

DI void k1_vstep(K1State& st, int slot, bool ISSUE, int jn, int sn) {
  unsigned a0 = st.aV + slot * K1_TILE, a8 = st.aV8 + slot * K1_TILE;
  asm volatile("" : "+v"(a0), "+v"(a8));
  s16x8 vf[2][4];
#pragma unroll
  for (int i = 0; i < 4; ++i) { const unsigned a = a0 ^ (i << 6); vf[0][i] = lds_tr2(a, (a ^ 32u) + 2048); }
#pragma unroll
  for (int ks = 0; ks < 4; ++ks) {
    if (ks < 3) {
#pragma unroll
      for (int i = 0; i < 4; ++i) { const unsigned a = (a0 ^ (i << 6)) + (ks + 1) * 4096; vf[(ks + 1) & 1][i] = lds_tr2(a, (a ^ 32u) + 2048); }
    } else {
#pragma unroll
      for (int i = 0; i < 4; ++i) vf[0][i] = lds_tr2(a8 ^ ((2 * i) << 5), a8 ^ ((2 * i + 1) << 5));
    }
#pragma unroll
    for (int i = 0; i < 4; ++i) st.o[i] = mfma32(vf[ks & 1][i], st.p[ks], st.o[i]);
    __builtin_amdgcn_sched_barrier(0);
    if (ISSUE) k1_issue_piece(st, jn, sn, ks);
    __builtin_amdgcn_sched_barrier(0);
  }
#pragma unroll
  for (int i = 0; i < 4; ++i) {
    s16x4 lo = __builtin_shufflevector(vf[0][i], vf[0][i], 0, 1, 2, 3), hi = __builtin_shufflevector(vf[0][i], vf[0][i], 4, 5, 6, 7);
    st.o8[2 * i] = mfma16k16(lo, st.p8, st.o8[2 * i]);
    st.o8[2 * i + 1] = mfma16k16(hi, st.p8, st.o8[2 * i + 1]);
  }
}

DI void k1_pair(K1State& st, int it, int& slot) {
  const int j = K1_NW + 2 * it;
  if (it == 0) wait_vm<8>(); else if (it < 15) wait_vm<12>(); else wait_vm<4>();
  bar();
  STAMP(0);
  if (it == 0) k1_issue_kv(st, j + 3, (K1_NW + 3) % K1_NSLOT);
  int s2 = slot + 4; if (s2 >= K1_NSLOT) s2 -= K1_NSLOT;
  k1_kstep(st, slot, it == 0, it < 14, j + 4, s2);
  slot = (slot + 1 == K1_NSLOT) ? 0 : slot + 1;
  STAMP(2);
  if (it < 14) wait_vm<12>(); else if (it == 14) wait_vm<8>(); else wait_vm<0>();
  bar();
  STAMP(3);
  s2 = slot + 4; if (s2 >= K1_NSLOT) s2 -= K1_NSLOT;
  k1_vstep(st, slot, it < 14, j + 5, s2);
  slot = (slot + 1 == K1_NSLOT) ? 0 : slot + 1;
  STAMP(4);
}

DI s16x8 k1_qfrag16(const f32x4& t0, const f32x4& t1, int srcA, int srcB, bool hiT) {
  unsigned p00 = cvtpk(t0[0], t0[1]), p01 = cvtpk(t0[2], t0[3]), p10 = cvtpk(t1[0], t1[1]), p11 = cvtpk(t1[2], t1[3]);
  u32x4 w;
  { unsigned a0 = __builtin_amdgcn_ds_bpermute(srcA, p00), a1 = __builtin_amdgcn_ds_bpermute(srcA, p10); w[0] = hiT ? a1 : a0; }
  { unsigned a0 = __builtin_amdgcn_ds_bpermute(srcA, p01), a1 = __builtin_amdgcn_ds_bpermute(srcA, p11); w[1] = hiT ? a1 : a0; }
  { unsigned a0 = __builtin_amdgcn_ds_bpermute(srcB, p00), a1 = __builtin_amdgcn_ds_bpermute(srcB, p10); w[2] = hiT ? a1 : a0; }
  { unsigned a0 = __builtin_amdgcn_ds_bpermute(srcB, p01), a1 = __builtin_amdgcn_ds_bpermute(srcB, p11); w[3] = hiT ? a1 : a0; }
  return __builtin_bit_cast(s16x8, w);
}

__global__ __launch_bounds__(K1_WAVES * 64, 2)
void mha_attn_kernel(const __hip_bfloat16* __restrict__ xA, const __hip_bfloat16* __restrict__ kA,
                     const __hip_bfloat16* __restrict__ vA, const __hip_bfloat16* __restrict__ wqA,
                     const __hip_bfloat16* __restrict__ xB, const __hip_bfloat16* __restrict__ kB,
                     const __hip_bfloat16* __restrict__ vB, const __hip_bfloat16* __restrict__ wqB,
                     const __hip_bfloat16* __restrict__ woA, const __hip_bfloat16* __restrict__ woB,
                     __hip_bfloat16* __restrict__ ows) {
  const int tid = threadIdx.x;
  const int wave = __builtin_amdgcn_readfirstlane(tid >> 6);
  const int lane = tid & 63;
  const int r16 = lane & 15, g = lane >> 4, r32 = lane & 31, hh = lane >> 5, blk = g & 1;
  const int bid = blockIdx.x;
  const int xcd = bid & 7, loc = bid >> 3;
  const int prob = xcd & 1, h = ((xcd >> 1) & 1) * 16 + (loc & 15), b = (xcd >> 2) * 4 + (loc >> 4);

  const char* xg = (const char*)(prob ? xB : xA) + (size_t)(b * SQ) * (DM * 2);
  const char* wg = (const char*)(prob ? wqB : wqA) + (size_t)h * (DH * 2);
  K1State st;
  st.kg = (const char*)(prob ? kB : kA) + (size_t)(b * NH + h) * (SK * DH * 2);
  st.vg = (const char*)(prob ? vB : vA) + (size_t)(b * NH + h) * (SK * DH * 2);
  st.ldsw = wave * 4096;
  st.rot = (h + 5 * b) & 15;
  unsigned offW[4];
#pragma unroll
  for (int j = 0; j < 4; ++j) {
    const int row = wave * 16 + j * 4 + g;
    const int fK = row & 15;
    const int fV = ((row & 3) << 2) | ((((row >> 2) ^ (row >> 3)) & 1) << 1);
    st.offK[j] = row * 256 + ((r16 ^ fK) << 4);
    st.offV[j] = row * 256 + ((r16 ^ fV) << 4);
    offW[j]    = row * (HD * 2) + ((r16 ^ fV) << 4);
  }
  const int qq = r16 >> 2, pp = r16 & 3;
  const unsigned pb = ((pp >> 1) << 4) + ((pp & 1) << 3);
  st.aK  = r32 * 256 + ((hh ^ (r32 & 15)) << 4);
  st.aK8 = (wave * 16 + r16) * 256 + ((g ^ r16) << 4);
  st.aV  = (4 * hh + qq) * 256 + (qq << 6) + ((blk ^ hh) << 5) + pb;
  st.aV8 = (wave * 16 + 4 * g + qq) * 256 + (qq << 6) + ((((g & 1) ^ (g >> 1))) << 5) + pb;
  const unsigned aW  = (8 * hh + qq) * 256 + (qq << 6) + ((blk ^ hh) << 5) + pb;
  const unsigned aW8 = ((8 * g + qq) * 256 + (qq << 6) + ((g & 1) << 5) + pb) ^ ((unsigned)wave << 6);
  const unsigned xoff8 = (128 + r16) * (DM * 2) + g * 16;
  unsigned offX[4];
#pragma unroll
  for (int j = 0; j < 4; ++j) {
    const int row = wave * 32 + j * 8 + (lane >> 3);
    offX[j] = row * (DM * 2) + (((lane & 7) ^ ((row >> 1) & 7)) << 4);
  }
  const unsigned aX = (wave * 32 + r32) * 128 + ((hh ^ ((r32 >> 1) & 7)) << 4);

  s16x8 xf8[3][2];
#define X_LD(DST, OFF, IMM) asm volatile("global_load_dwordx4 %0, %1, %2 offset:%3" : "=v"(DST) : "v"(OFF), "s"(xg), "i"(IMM) : "memory")
#define X8_LOAD(T) do { X_LD(xf8[(T) % 3][0], xoff8, (T) * 128); X_LD(xf8[(T) % 3][1], xoff8, (T) * 128 + 64); } while (0)
#define WSAME 0
#define XSAME 0
#define DMA_WX dma16
#define T_PIECE(N, I) do { const unsigned lb_ = ((N) % K1_NSLOT) * K1_TILE + st.ldsw + (I) * 1024; \
    if ((N) >= K1_NW) { k1_issue_piece(st, (N), (N) % K1_NSLOT, (I)); } \
    else if ((N) & 1) DMA_WX(lb_, offW[I], wg + (size_t)(WSAME ? 0 : ((N) >> 1)) * 64 * (HD * 2)); \
    else DMA_WX(lb_, offX[I], xg + (XSAME ? 0 : ((N) >> 1)) * 128); } while (0)
#define T_ISSUE(N) do { T_PIECE(N, 0); T_PIECE(N, 1); T_PIECE(N, 2); T_PIECE(N, 3); } while (0)

  u32x4 wop[5];
  {
    const char* wo = (const char*)((bid & 1) ? woB : woA) + (size_t)(bid >> 1) * 20480;
#pragma unroll
    for (int i = 0; i < 5; ++i) asm volatile("global_load_dwordx4 %0, %1, %2 offset:%3" : "=v"(wop[i]) : "v"((unsigned)(tid * 16)), "s"(wo + i * 4096), "i"(0) : "memory");
  }
  unsigned dummy = 0;
  T_ISSUE(0); X8_LOAD(0); T_ISSUE(1); T_ISSUE(2); X8_LOAD(1);

  f32x16 qa[4]; f32x4 qa8[2];
#pragma unroll
  for (int dt = 0; dt < 4; ++dt)
#pragma unroll
    for (int r = 0; r < 16; ++r) qa[dt][r] = 0.f;
  qa8[0] = f32x4{0.f, 0.f, 0.f, 0.f}; qa8[1] = f32x4{0.f, 0.f, 0.f, 0.f};

#pragma unroll
  for (int t = 0; t < K1_NWS; ++t) {
    if (t + 1 <= 9) wait_vm<6>(); else wait_vm<4>();
    asm volatile("" : "+v"(xf8[t % 3][0]), "+v"(xf8[t % 3][1]), "+v"(dummy));
    if (t == 0) asm volatile("" : "+v"(wop[0]), "+v"(wop[1]), "+v"(wop[2]), "+v"(wop[3]), "+v"(wop[4]));
    bar();
    STAMP(5);
    unsigned a0 = aW + ((2 * t + 1) % K1_NSLOT) * K1_TILE, a8 = aW8 + ((2 * t + 1) % K1_NSLOT) * K1_TILE, ax = aX + ((2 * t) % K1_NSLOT) * K1_TILE;
    asm volatile("" : "+v"(a0), "+v"(a8), "+v"(ax));
    {
      s16x8 wf[2][4], xf[4];
#pragma unroll
      for (int kk = 0; kk < 4; ++kk) xf[kk] = lds_b128(ax ^ (kk << 5));
#pragma unroll
      for (int i = 0; i < 4; ++i) { const unsigned a = a0 ^ (i << 6); wf[0][i] = lds_tr2(a, (a ^ 32u) + 1024); }
#pragma unroll
      for (int kk = 0; kk < 4; ++kk) {
        if (kk < 3) {
#pragma unroll
          for (int i = 0; i < 4; ++i) { const unsigned a = (a0 ^ (i << 6)) + (kk + 1) * 4096; wf[(kk + 1) & 1][i] = lds_tr2(a, (a ^ 32u) + 1024); }
        } else {
#pragma unroll
          for (int i = 0; i < 4; ++i) { const unsigned a = (a8 ^ ((i & 1) << 5)) + (i >> 1) * 8192; wf[0][i] = lds_tr2(a, (a ^ 32u) + 1024); }
        }
#pragma unroll
        for (int i = 0; i < 4; ++i) qa[i] = mfma32(wf[kk & 1][i], xf[kk], qa[i]);
        __builtin_amdgcn_sched_barrier(0);
        T_PIECE(2 * t + 3 + (kk >> 1), 2 * (kk & 1)); T_PIECE(2 * t + 3 + (kk >> 1), 2 * (kk & 1) + 1);
        if (kk == 3 && t + 2 < K1_NWS) X8_LOAD(t + 2);
        __builtin_amdgcn_sched_barrier(0);
      }
#pragma unroll
      for (int i = 0; i < 4; ++i) qa8[i & 1] = mfma16(wf[0][i], xf8[t % 3][i >> 1], qa8[i & 1]);
    }
    STAMP(6);
  }
  {
#pragma unroll
    for (int s = 0; s < 8; ++s) {
      const f32x16& a = qa[s >> 1]; const int b0 = 8 * (s & 1);
      u32x4 w = pack_swap(a[b0] * SC_L2E, a[b0 + 1] * SC_L2E, a[b0 + 2] * SC_L2E, a[b0 + 3] * SC_L2E,
                          a[b0 + 4] * SC_L2E, a[b0 + 5] * SC_L2E, a[b0 + 6] * SC_L2E, a[b0 + 7] * SC_L2E);
      st.q[s] = __builtin_bit_cast(s16x8, w);
    }
    const int srcA = (r16 + 16 * (2 * (g & 1))) << 2, srcB = srcA + 64;
    const bool hiT = (g >> 1) != 0;
    s16x8 q8w = k1_qfrag16(qa8[0] * SC_L2E, qa8[1] * SC_L2E, srcA, srcB, hiT);
    const unsigned sc = ((K1_NW - 1) % K1_NSLOT) * K1_TILE;
    asm volatile("s_waitcnt lgkmcnt(0)" ::: "memory"); bar();
    *(__attribute__((address_space(3))) s16x8*)(uintptr_t)(sc + wave * 1024 + lane * 16) = q8w;
    asm volatile("s_waitcnt lgkmcnt(0)" ::: "memory"); bar();
#pragma unroll
    for (int s = 0; s < 4; ++s) st.q8[s] = lds_b128(sc + s * 1024 + lane * 16);
    asm volatile("s_waitcnt lgkmcnt(0)" ::: "memory");
  }
#pragma unroll
  for (int dt = 0; dt < 4; ++dt)
#pragma unroll
    for (int r = 0; r < 16; ++r) st.o[dt][r] = 0.f;
#pragma unroll
  for (int dt = 0; dt < 8; ++dt) st.o8[dt] = f32x4{0.f, 0.f, 0.f, 0.f};
#pragma unroll
  for (int r = 0; r < 16; ++r) st.negm[r] = 0.f;
  st.negm8 = f32x4{0.f, 0.f, 0.f, 0.f};
  st.m = st.m8 = 0.f; st.l = st.l8 = 0.f;

  int slot = K1_NW % K1_NSLOT;
#pragma unroll 1
  for (int it = 0; it < 16; ++it) k1_pair(st, it, slot);

  int tz = threadIdx.x; asm volatile("" : "+v"(tz));
  char* ob = (char*)ows + ((size_t)(prob * MROWS + b * SQ) * HD + h * DH) * 2;
  {
    const float inv = 1.f / xsum32(st.l);
    char* og = ob + (size_t)(wave * 32 + (tz & 31)) * (HD * 2) + ((tz >> 5) & 1) * 16;
#pragma unroll
    for (int dt = 0; dt < 4; ++dt)
#pragma unroll
      for (int s2 = 0; s2 < 2; ++s2) {
        const f32x16& a = st.o[dt]; const int b0 = 8 * s2;
        u32x4 w = pack_swap(a[b0] * inv, a[b0 + 1] * inv, a[b0 + 2] * inv, a[b0 + 3] * inv, a[b0 + 4] * inv, a[b0 + 5] * inv, a[b0 + 6] * inv, a[b0 + 7] * inv);
        *(u32x4*)(og + dt * 64 + s2 * 32) = w;
      }
  }
  {
    typedef __attribute__((address_space(3))) f32x4* lds_f32x4_ptr;
    typedef __attribute__((address_space(3))) float* lds_f32_ptr;
    const int l2 = tz & 63, r = tz & 15;
    const float l8t = xsum16_32(st.l8);
    asm volatile("s_waitcnt lgkmcnt(0)" ::: "memory"); bar();
#pragma unroll
    for (int dt = 0; dt < 8; ++dt) *(lds_f32x4_ptr)(uintptr_t)(wave * 8192 + dt * 1024 + l2 * 16) = st.o8[dt];
    if (l2 < 16) { *(lds_f32_ptr)(uintptr_t)(32768 + (wave * 16 + r) * 4) = st.m8; *(lds_f32_ptr)(uintptr_t)(32768 + 256 + (wave * 16 + r) * 4) = l8t; }
    asm volatile("s_waitcnt lgkmcnt(0)" ::: "memory"); bar();
    float mw[4], lw[4];
#pragma unroll
    for (int w = 0; w < 4; ++w) { mw[w] = *(lds_f32_ptr)(uintptr_t)(32768 + (w * 16 + r) * 4); lw[w] = *(lds_f32_ptr)(uintptr_t)(32768 + 256 + (w * 16 + r) * 4); }
    const float M = fmaxf(fmaxf(mw[0], mw[1]), fmaxf(mw[2], mw[3]));
    float L = 0.f, sc[4];
#pragma unroll
    for (int w = 0; w < 4; ++w) { sc[w] = __builtin_amdgcn_exp2f(mw[w] - M); L += lw[w] * sc[w]; }
    const float inv = 1.f / L;
    char* og = ob + (size_t)(128 + r) * (HD * 2) + ((l2 >> 4) & 3) * 8;
#pragma unroll
    for (int d2 = 0; d2 < 2; ++d2) {
      const int dt = 2 * wave + d2;
      f32x4 acc = {0.f, 0.f, 0.f, 0.f};
#pragma unroll
      for (int w = 0; w < 4; ++w) { f32x4 v = *(lds_f32x4_ptr)(uintptr_t)(w * 8192 + dt * 1024 + l2 * 16); acc += v * sc[w]; }
      u32x2 wv = {cvtpk(acc[0] * inv, acc[1] * inv), cvtpk(acc[2] * inv, acc[3] * inv)};
      *(u32x2*)(og + dt * 32) = wv;
    }
  }
}

constexpr int K2_BM = 96, K2_BN = 64, K2_BK = 128;
constexpr int K2_OT = K2_BM * K2_BK * 2;
constexpr int K2_WT = K2_BK * K2_BN * 2;
constexpr int K2_ST = K2_OT + K2_WT;
#define K2NS 4
constexpr int K2_NS = K2NS;
constexpr int K2_LDS = K2_NS * K2_ST;
constexpr int K2_STEPS = HD / K2_BK;
constexpr int K2_NLD = 4;
constexpr int K2_THREADS = (8 + K2_NLD) * 64;

__global__ __launch_bounds__(K2_THREADS, 3)
void mha_oproj_kernel(const __hip_bfloat16* __restrict__ ows, const __hip_bfloat16* __restrict__ woA,
                      const __hip_bfloat16* __restrict__ woB, __hip_bfloat16* __restrict__ out) {
  const int tid = threadIdx.x;
  const int wave = __builtin_amdgcn_readfirstlane(tid >> 6);
  const int lane = tid & 63;
  const int r16 = lane & 15, g = lane >> 4;
  const int bid = blockIdx.x;
  const int xcd = bid & 7, loc = bid >> 3;
  const int mblk = xcd * 3 + loc / 10, nblk = loc % 10;
  const int prob = mblk / 12, mb = mblk % 12;
  if (wave >= 8) {
    const int lw = wave - 8;
    const char* og = (const char*)ows + (size_t)(prob * MROWS + mb * K2_BM) * (HD * 2);
    const char* wg = (const char*)(prob ? woB : woA) + (size_t)nblk * (K2_BN * 2);
    const int rot = 0;
    unsigned offO[6], offWo[4];
#pragma unroll
    for (int j = 0; j < 6; ++j) { const int row = lw * 24 + j * 4 + g; offO[j] = row * (HD * 2) + ((r16 ^ (row & 15)) << 4); }
#pragma unroll
    for (int j = 0; j < 4; ++j) {
      const int row = lw * 32 + j * 8 + (lane >> 3);
      offWo[j] = row * (DM * 2) + (((lane & 7) ^ (((row >> 1) & 1) << 2)) << 4);
    }
#define K2_DMA_O dma16
#define K2_DMA_W dma16
#define K2_ISSUE(T, SL) do { const char* go = og + (size_t)(T) * (K2_BK * 2); const char* gw = wg + (size_t)(T) * K2_BK * (DM * 2); \
    const unsigned lb = (SL) * K2_ST; \
    _Pragma("unroll") for (int j_ = 0; j_ < 6; ++j_) K2_DMA_O(lb + lw * 6144 + j_ * 1024, offO[j_], go); \
    _Pragma("unroll") for (int j_ = 0; j_ < 4; ++j_) K2_DMA_W(lb + K2_OT + lw * 4096 + j_ * 1024, offWo[j_], gw); } while (0)
#pragma unroll
    for (int i = 0; i < K2_NS - 1; ++i) K2_ISSUE((i + rot) & 31, i);
    int slot = 0;
#pragma unroll 1
    for (int t = 0; t < K2_STEPS; ++t) {
      if (t + K2_NS - 2 < K2_STEPS) wait_vm<10 * (K2_NS - 2)>();
      else if (K2_NS > 3 && t + K2_NS - 3 < K2_STEPS) wait_vm<(K2_NS > 3 ? 10 * (K2_NS - 3) : 0)>();
      else wait_vm<0>();
      bar();
      if (t + K2_NS - 1 < K2_STEPS) { int s2 = slot + K2_NS - 1; if (s2 >= K2_NS) s2 -= K2_NS; K2_ISSUE((t + K2_NS - 1 + rot) & 31, s2); }
      slot = (slot + 1 == K2_NS) ? 0 : slot + 1;
    }
    bar(); bar();
    return;
  }
  const int kq = wave >> 1, wn = wave & 1;
  const int r32 = lane & 31, hh = lane >> 5, blk = g & 1, qq = r16 >> 2, pp = r16 & 3;
  const unsigned aO = r32 * 256 + (((4 * kq + hh) ^ (r32 & 15)) << 4);
  unsigned aWo;
  {
    const int rowl = 8 * hh + qq;
    aWo = K2_OT + (32 * kq + rowl) * 128 + (((4 * wn + 2 * blk + (pp >> 1)) ^ (((rowl >> 1) & 1) << 2)) << 4) + ((pp & 1) << 3);
  }
  f32x16 acc[3];
#pragma unroll
  for (int i = 0; i < 3; ++i)
#pragma unroll
    for (int r = 0; r < 16; ++r) acc[i][r] = 0.f;
  int slot = 0;
#pragma unroll 1
  for (int t = 0; t < K2_STEPS; ++t) {
    bar();
    const unsigned sb = slot * K2_ST;
    s16x8 wf[2], of[2][3];
#pragma unroll
    for (int s2 = 0; s2 < 2; ++s2) {
      const unsigned aw = aWo + sb + s2 * 16 * 128;
      wf[s2] = lds_tr2(aw, aw + 512);
      const unsigned ao = (aO ^ (s2 << 5)) + sb;
#pragma unroll
      for (int mt = 0; mt < 3; ++mt) of[s2][mt] = lds_b128(ao + mt * 8192);
    }
#pragma unroll
    for (int s2 = 0; s2 < 2; ++s2)
#pragma unroll
      for (int mt = 0; mt < 3; ++mt) acc[mt] = mfma32(wf[s2], of[s2][mt], acc[mt]);
    slot = (slot + 1 == K2_NS) ? 0 : slot + 1;
  }
  {
    typedef __attribute__((address_space(3))) f32x4* lds_f32x4_ptr;
    bar();
    if (kq > 0) {
#pragma unroll
      for (int mt = 0; mt < 3; ++mt)
#pragma unroll
        for (int q4 = 0; q4 < 4; ++q4)
          *(lds_f32x4_ptr)(uintptr_t)((((kq - 1) * 2 + wn) * 12 + mt * 4 + q4) * 1024 + lane * 16) = f32x4{acc[mt][4 * q4], acc[mt][4 * q4 + 1], acc[mt][4 * q4 + 2], acc[mt][4 * q4 + 3]};
    }
    bar();
    if (kq == 0) {
#pragma unroll
      for (int mt = 0; mt < 3; ++mt) {
#pragma unroll
        for (int q4 = 0; q4 < 4; ++q4)
#pragma unroll
          for (int k2 = 0; k2 < 3; ++k2) {
            f32x4 v = *(lds_f32x4_ptr)(uintptr_t)(((k2 * 2 + wn) * 12 + mt * 4 + q4) * 1024 + lane * 16);
            acc[mt][4 * q4] += v[0]; acc[mt][4 * q4 + 1] += v[1]; acc[mt][4 * q4 + 2] += v[2]; acc[mt][4 * q4 + 3] += v[3];
          }
        const int row = mb * K2_BM + mt * 32 + r32;
        char* op = (char*)out + ((size_t)row * (2 * DM) + prob * DM + nblk * K2_BN + wn * 32 + hh * 8) * 2;
#pragma unroll
        for (int s2 = 0; s2 < 2; ++s2) {
          const int b0 = 8 * s2;
          u32x4 w = pack_swap(acc[mt][b0], acc[mt][b0 + 1], acc[mt][b0 + 2], acc[mt][b0 + 3], acc[mt][b0 + 4], acc[mt][b0 + 5], acc[mt][b0 + 6], acc[mt][b0 + 7]);
          *(u32x4*)(op + s2 * 32) = w;
        }
      }
    }
  }
}

extern "C" void kernel_launch(void* const* d_in, const int* in_sizes, int n_in,
                              void* d_out, int out_size, void* d_ws, size_t ws_size,
                              hipStream_t stream) {
  const __hip_bfloat16* xA  = (const __hip_bfloat16*)d_in[0];
  const __hip_bfloat16* kA  = (const __hip_bfloat16*)d_in[1];
  const __hip_bfloat16* vA  = (const __hip_bfloat16*)d_in[2];
  const __hip_bfloat16* wqA = (const __hip_bfloat16*)d_in[3];
  const __hip_bfloat16* woA = (const __hip_bfloat16*)d_in[4];
  const __hip_bfloat16* xB  = (const __hip_bfloat16*)d_in[5];
  const __hip_bfloat16* kB  = (const __hip_bfloat16*)d_in[6];
  const __hip_bfloat16* vB  = (const __hip_bfloat16*)d_in[7];
  const __hip_bfloat16* wqB = (const __hip_bfloat16*)d_in[8];
  const __hip_bfloat16* woB = (const __hip_bfloat16*)d_in[9];
  __hip_bfloat16* ows = (__hip_bfloat16*)d_ws;
  (void)hipFuncSetAttribute((const void*)mha_attn_kernel, hipFuncAttributeMaxDynamicSharedMemorySize, K1_LDS);
  (void)hipFuncSetAttribute((const void*)mha_oproj_kernel, hipFuncAttributeMaxDynamicSharedMemorySize, K2_LDS);
  hipLaunchKernelGGL(mha_attn_kernel, dim3(512), dim3(K1_WAVES * 64), K1_LDS, stream, xA, kA, vA, wqA, xB, kB, vB, wqB, woA, woB, ows);
  hipLaunchKernelGGL(mha_oproj_kernel, dim3(240), dim3(K2_THREADS), K2_LDS, stream, (const __hip_bfloat16*)ows, woA, woB, (__hip_bfloat16*)d_out);
}
```

```cpp
#include <hip/hip_runtime.h>
#include <hip/hip_bf16.h>
#include <stdint.h>

typedef short    s16x4 __attribute__((ext_vector_type(4)));
typedef short    s16x8 __attribute__((ext_vector_type(8)));
typedef float    f32x4 __attribute__((ext_vector_type(4)));
typedef float    f32x2 __attribute__((ext_vector_type(2)));
typedef __bf16   bf16x2 __attribute__((ext_vector_type(2)));
typedef unsigned u32x4 __attribute__((ext_vector_type(4)));
typedef unsigned u32x2 __attribute__((ext_vector_type(2)));
typedef __attribute__((address_space(3))) s16x4* lds_s16x4_ptr;
typedef __attribute__((address_space(3))) const s16x8* lds_s16x8_ptr;
#define DI __device__ __forceinline__

constexpr int NB = 8, SQ = 144, DM = 640, NH = 32, SK = 1024, DH = 128;
constexpr int HD = NH * DH;
constexpr int MROWS = NB * SQ;

DI unsigned cvtpk(float lo, float hi) {
  f32x2 v = {lo, hi};
  bf16x2 b = __builtin_convertvector(v, bf16x2);
  return __builtin_bit_cast(unsigned, b);
}
DI f32x4 mfma16(s16x8 a, s16x8 b, f32x4 c) {
  return __builtin_amdgcn_mfma_f32_16x16x32_bf16(a, b, c, 0, 0, 0);
}
DI void dma16(unsigned lds_addr, unsigned voff, const char* gbase) {
  asm volatile("s_mov_b32 m0, %0\n\ts_nop 0\n\tglobal_load_lds_dwordx4 %1, %2"
               :: "s"(lds_addr), "v"(voff), "s"(gbase) : "memory");
}
DI void dma16nt(unsigned lds_addr, unsigned voff, const char* gbase) {
  asm volatile("s_mov_b32 m0, %0\n\ts_nop 0\n\tglobal_load_lds_dwordx4 %1, %2 nt"
               :: "s"(lds_addr), "v"(voff), "s"(gbase) : "memory");
}
template <int N> DI void wait_vm() { asm volatile("s_waitcnt vmcnt(%0)" :: "i"(N) : "memory"); }
DI void bar() { asm volatile("s_waitcnt lgkmcnt(0)\n\ts_barrier" ::: "memory"); }
DI s16x8 lds_b128(unsigned addr) { return *(lds_s16x8_ptr)(uintptr_t)addr; }
DI s16x8 lds_tr2(unsigned addr0, unsigned addr1) {
  s16x4 lo = __builtin_amdgcn_ds_read_tr16_b64_v4i16((lds_s16x4_ptr)(uintptr_t)addr0);
  s16x4 hi = __builtin_amdgcn_ds_read_tr16_b64_v4i16((lds_s16x4_ptr)(uintptr_t)addr1);
  return __builtin_shufflevector(lo, hi, 0, 1, 2, 3, 4, 5, 6, 7);
}
DI float xmax16_32(float v) {
  auto r = __builtin_amdgcn_permlane32_swap(__float_as_uint(v), __float_as_uint(v), false, false);
  v = fmaxf(__uint_as_float(r[0]), __uint_as_float(r[1]));
  auto r2 = __builtin_amdgcn_permlane16_swap(__float_as_uint(v), __float_as_uint(v), false, false);
  return fmaxf(__uint_as_float(r2[0]), __uint_as_float(r2[1]));
}
DI float xsum16_32(float v) {
  auto r = __builtin_amdgcn_permlane32_swap(__float_as_uint(v), __float_as_uint(v), false, false);
  v = __uint_as_float(r[0]) + __uint_as_float(r[1]);
  auto r2 = __builtin_amdgcn_permlane16_swap(__float_as_uint(v), __float_as_uint(v), false, false);
  return __uint_as_float(r2[0]) + __uint_as_float(r2[1]);
}

typedef float f32x16 __attribute__((ext_vector_type(16)));
constexpr int K1_WAVES = 4;
constexpr int K1_NSLOT = 5;
constexpr int K1_TILE  = 16384;
constexpr int K1_NWS   = 10;
constexpr int K1_NW    = 20;
constexpr int K1_LDS   = K1_NSLOT * K1_TILE;
constexpr float SC_L2E = 0.088388347648318447f * 1.4426950408889634f;
constexpr float K1_THR = 8.0f;

DI f32x4 mfma16k16(s16x4 a, s16x4 b, f32x4 c) { return __builtin_amdgcn_mfma_f32_16x16x16bf16_1k(a, b, c, 0, 0, 0); }
DI f32x16 mfma32(s16x8 a, s16x8 b, f32x16 c) { return __builtin_amdgcn_mfma_f32_32x32x16_bf16(a, b, c, 0, 0, 0); }
DI s16x4 lds_tr(unsigned addr) { return __builtin_amdgcn_ds_read_tr16_b64_v4i16((lds_s16x4_ptr)(uintptr_t)addr); }
DI float max3f(float a, float b, float c) { float r; asm("v_max3_f32 %0, %1, %2, %3" : "=v"(r) : "v"(a), "v"(b), "v"(c)); return r; }
DI float max2f(float a, float b) { float r; asm("v_max_f32_e32 %0, %1, %2" : "=v"(r) : "v"(a), "v"(b)); return r; }
DI float xmax32(float v) {
  auto r = __builtin_amdgcn_permlane32_swap(__float_as_uint(v), __float_as_uint(v), false, false);
  return max2f(__uint_as_float(r[0]), __uint_as_float(r[1]));
}
DI float xmax16(float v) {
  auto r = __builtin_amdgcn_permlane16_swap(__float_as_uint(v), __float_as_uint(v), false, false);
  return max2f(__uint_as_float(r[0]), __uint_as_float(r[1]));
}
DI float xsum32(float v) {
  auto r = __builtin_amdgcn_permlane32_swap(__float_as_uint(v), __float_as_uint(v), false, false);
  return __uint_as_float(r[0]) + __uint_as_float(r[1]);
}
DI u32x4 pack_swap(float a0, float a1, float a2, float a3, float b0, float b1, float b2, float b3) {
  unsigned A0 = cvtpk(a0, a1), A1 = cvtpk(a2, a3), B0 = cvtpk(b0, b1), B1 = cvtpk(b2, b3);
  auto r0 = __builtin_amdgcn_permlane32_swap(A0, B0, false, false);
  auto r1 = __builtin_amdgcn_permlane32_swap(A1, B1, false, false);
  return u32x4{r0[0], r1[0], r0[1], r1[1]};
}

#define STAMP(i) do {} while (0)
struct K1State {
  unsigned offK[4], offV[4];
  const char *kg, *vg;
  unsigned ldsw;
  unsigned aK, aV, aK8, aV8;
  int rot;
  s16x8 q[8], q8[4];
  f32x16 o[4]; f32x4 o8[8];
  f32x16 negm; f32x4 negm8;
  float m, l, m8, l8;
  s16x8 p[4];
  s16x4 p8;
};

DI int k1_phys(int lg) { return (0x40321 >> (4 * lg)) & 7; }
DI void k1_issue_piece(K1State& st, int n, int slot, int i) {
  const int t = ((n >> 1) + st.rot) & 15;
  const unsigned lb = slot * K1_TILE + st.ldsw + i * 1024;
  if (n & 1) dma16nt(lb, st.offV[i], st.vg + (size_t)t * K1_TILE);
  else       dma16nt(lb, st.offK[i], st.kg + (size_t)t * K1_TILE);
}
DI void k1_issue_kv(K1State& st, int n, int slot) {
#pragma unroll
  for (int i = 0; i < 4; ++i) k1_issue_piece(st, n, slot, i);
}

DI void k1_kstep(K1State& st, int slot, bool FIRST, bool ISSUE, int jn, int sn) {
  unsigned a0 = st.aK + slot * K1_TILE, a8 = st.aK8 + slot * K1_TILE;
  asm volatile("" : "+v"(a0), "+v"(a8));
  f32x16 s[2]; f32x4 s8;
  s16x8 kf[3][2], k8[2];
#pragma unroll
  for (int i = 0; i < 2; ++i) { kf[i][0] = lds_b128(a0 ^ (i << 5)); kf[i][1] = lds_b128((a0 ^ (i << 5)) + 8192); }
  k8[0] = lds_b128(a8);
#pragma unroll
  for (int sg = 0; sg < 8; ++sg) {
    if (sg < 6) { kf[(sg + 2) % 3][0] = lds_b128(a0 ^ ((sg + 2) << 5)); kf[(sg + 2) % 3][1] = lds_b128((a0 ^ ((sg + 2) << 5)) + 8192); }
    if ((sg & 1) == 0 && sg < 6) k8[((sg >> 1) + 1) & 1] = lds_b128(a8 ^ (((sg >> 1) + 1) << 6));
    if (sg == 0) {
      s[0] = mfma32(kf[0][0], st.q[0], st.negm);
      s[1] = mfma32(kf[0][1], st.q[0], st.negm);
      s8 = mfma16(k8[0], st.q8[0], st.negm8);
    } else {
      s[0] = mfma32(kf[sg % 3][0], st.q[sg], s[0]);
      s[1] = mfma32(kf[sg % 3][1], st.q[sg], s[1]);
      if ((sg & 1) == 0) s8 = mfma16(k8[(sg >> 1) & 1], st.q8[sg >> 1], s8);
    }
    __builtin_amdgcn_sched_barrier(0);
    if ((sg & 1) == 1 && ISSUE) k1_issue_piece(st, jn, sn, sg >> 1);
    __builtin_amdgcn_sched_barrier(0);
  }
  STAMP(1);
  float t0 = max3f(s[0][0], s[0][1], s[0][2]), t1 = max3f(s[1][0], s[1][1], s[1][2]);
#pragma unroll
  for (int r = 3; r < 15; r += 2) { t0 = max3f(t0, s[0][r], s[0][r + 1]); t1 = max3f(t1, s[1][r], s[1][r + 1]); }
  float tm = max3f(t0, t1, max2f(s[0][15], s[1][15]));
  tm = xmax32(tm);
  float t8 = max2f(max2f(s8[0], s8[1]), max2f(s8[2], s8[3]));
  t8 = xmax16(xmax32(t8));
  if (FIRST || __any((tm > K1_THR) | (t8 > K1_THR))) {
    const float dl = FIRST ? tm : max2f(tm, 0.f);
    const float d8 = FIRST ? t8 : max2f(t8, 0.f);
    st.m += dl; st.m8 += d8;
#pragma unroll
    for (int r = 0; r < 16; ++r) { s[0][r] -= dl; s[1][r] -= dl; st.negm[r] = -st.m; }
#pragma unroll
    for (int r = 0; r < 4; ++r) { s8[r] -= d8; st.negm8[r] = -st.m8; }
    {
      const float f = FIRST ? 0.f : __builtin_amdgcn_exp2f(-dl), f8 = FIRST ? 0.f : __builtin_amdgcn_exp2f(-d8);
      st.l *= f; st.l8 *= f8;
#pragma unroll
      for (int dt = 0; dt < 4; ++dt)
#pragma unroll
        for (int r = 0; r < 16; ++r) st.o[dt][r] *= f;
#pragma unroll
      for (int dt = 0; dt < 8; ++dt)
#pragma unroll
        for (int r = 0; r < 4; ++r) st.o8[dt][r] *= f8;
    }
  }
  float ps0 = 0.f, ps1 = 0.f, ps8 = 0.f;
#pragma unroll
  for (int r = 0; r < 16; ++r) {
    float e0 = __builtin_amdgcn_exp2f(s[0][r]), e1 = __builtin_amdgcn_exp2f(s[1][r]);
    s[0][r] = e0; s[1][r] = e1; ps0 += e0; ps1 += e1;
  }
#pragma unroll
  for (int r = 0; r < 4; ++r) { float e = __builtin_amdgcn_exp2f(s8[r]); s8[r] = e; ps8 += e; }
  st.l += ps0 + ps1; st.l8 += ps8;
#pragma unroll
  for (int ks = 0; ks < 4; ++ks) {
    const int kt = ks >> 1, b = 8 * (ks & 1);
    u32x4 w = {cvtpk(s[kt][b], s[kt][b + 1]), cvtpk(s[kt][b + 2], s[kt][b + 3]), cvtpk(s[kt][b + 4], s[kt][b + 5]), cvtpk(s[kt][b + 6], s[kt][b + 7])};
    st.p[ks] = __builtin_bit_cast(s16x8, w);
  }
  u32x2 w8 = {cvtpk(s8[0], s8[1]), cvtpk(s8[2], s8[3])};
  st.p8 = __builtin_bit_cast(s16x4, w8);
}

DI void k1_vstep(K1State& st, int slot, bool ISSUE, int jn, int sn) {
  unsigned a0 = st.aV + slot * K1_TILE, a8 = st.aV8 + slot * K1_TILE;
  asm volatile("" : "+v"(a0), "+v"(a8));
  s16x8 vf[2][4];
#pragma unroll
  for (int i = 0; i < 4; ++i) { const unsigned a = a0 ^ (i << 6); vf[0][i] = lds_tr2(a, (a ^ 32u) + 2048); }
#pragma unroll
  for (int ks = 0; ks < 4; ++ks) {
    if (ks < 3) {
#pragma unroll
      for (int i = 0; i < 4; ++i) { const unsigned a = (a0 ^ (i << 6)) + (ks + 1) * 4096; vf[(ks + 1) & 1][i] = lds_tr2(a, (a ^ 32u) + 2048); }
    } else {
#pragma unroll
      for (int i = 0; i < 4; ++i) vf[0][i] = lds_tr2(a8 ^ ((2 * i) << 5), a8 ^ ((2 * i + 1) << 5));
    }
#pragma unroll
    for (int i = 0; i < 4; ++i) st.o[i] = mfma32(vf[ks & 1][i], st.p[ks], st.o[i]);
    __builtin_amdgcn_sched_barrier(0);
    if (ISSUE) k1_issue_piece(st, jn, sn, ks);
    __builtin_amdgcn_sched_barrier(0);
  }
#pragma unroll
  for (int i = 0; i < 4; ++i) {
    s16x4 lo = __builtin_shufflevector(vf[0][i], vf[0][i], 0, 1, 2, 3), hi = __builtin_shufflevector(vf[0][i], vf[0][i], 4, 5, 6, 7);
    st.o8[2 * i] = mfma16k16(lo, st.p8, st.o8[2 * i]);
    st.o8[2 * i + 1] = mfma16k16(hi, st.p8, st.o8[2 * i + 1]);
  }
}

DI void k1_pair(K1State& st, int it, int& lg) {
  const int n = 2 * it;
  if (it == 0) wait_vm<8>(); else if (it < 15) wait_vm<12>(); else wait_vm<4>();
  bar();
  STAMP(0);
  if (it == 0) k1_issue_kv(st, 3, 0);
  int l4 = lg + 4; if (l4 >= 5) l4 -= 5;
  k1_kstep(st, k1_phys(lg), it == 0, it < 14, n + 4, k1_phys(l4));
  lg = (lg == 4) ? 0 : lg + 1;
  STAMP(2);
  if (it < 14) wait_vm<12>(); else if (it == 14) wait_vm<8>(); else wait_vm<0>();
  bar();
  STAMP(3);
  l4 = lg + 4; if (l4 >= 5) l4 -= 5;
  k1_vstep(st, k1_phys(lg), it < 14, n + 5, k1_phys(l4));
  lg = (lg == 4) ? 0 : lg + 1;
  STAMP(4);
}

DI s16x8 k1_qfrag16(const f32x4& t0, const f32x4& t1, int srcA, int srcB, bool hiT) {
  unsigned p00 = cvtpk(t0[0], t0[1]), p01 = cvtpk(t0[2], t0[3]), p10 = cvtpk(t1[0], t1[1]), p11 = cvtpk(t1[2], t1[3]);
  u32x4 w;
  { unsigned a0 = __builtin_amdgcn_ds_bpermute(srcA, p00), a1 = __builtin_amdgcn_ds_bpermute(srcA, p10); w[0] = hiT ? a1 : a0; }
  { unsigned a0 = __builtin_amdgcn_ds_bpermute(srcA, p01), a1 = __builtin_amdgcn_ds_bpermute(srcA, p11); w[1] = hiT ? a1 : a0; }
  { unsigned a0 = __builtin_amdgcn_ds_bpermute(srcB, p00), a1 = __builtin_amdgcn_ds_bpermute(srcB, p10); w[2] = hiT ? a1 : a0; }
  { unsigned a0 = __builtin_amdgcn_ds_bpermute(srcB, p01), a1 = __builtin_amdgcn_ds_bpermute(srcB, p11); w[3] = hiT ? a1 : a0; }
  return __builtin_bit_cast(s16x8, w);
}

__global__ __launch_bounds__(K1_WAVES * 64, 2)
void mha_attn_kernel(const __hip_bfloat16* __restrict__ xA, const __hip_bfloat16* __restrict__ kA,
                     const __hip_bfloat16* __restrict__ vA, const __hip_bfloat16* __restrict__ wqA,
                     const __hip_bfloat16* __restrict__ xB, const __hip_bfloat16* __restrict__ kB,
                     const __hip_bfloat16* __restrict__ vB, const __hip_bfloat16* __restrict__ wqB,
                     const __hip_bfloat16* __restrict__ woA, const __hip_bfloat16* __restrict__ woB,
                     __hip_bfloat16* __restrict__ ows) {
  const int tid = threadIdx.x;
  const int wave = __builtin_amdgcn_readfirstlane(tid >> 6);
  const int lane = tid & 63;
  const int r16 = lane & 15, g = lane >> 4, r32 = lane & 31, hh = lane >> 5, blk = g & 1;
  const int bid = blockIdx.x;
  const int xcd = bid & 7, loc = bid >> 3;
  const int prob = xcd & 1, h = ((xcd >> 1) & 1) * 16 + (loc & 15), b = (xcd >> 2) * 4 + (loc >> 4);

  const char* xg = (const char*)(prob ? xB : xA) + (size_t)(b * SQ) * (DM * 2);
  const char* wg = (const char*)(prob ? wqB : wqA) + (size_t)h * (DH * 2);
  K1State st;
  st.kg = (const char*)(prob ? kB : kA) + (size_t)(b * NH + h) * (SK * DH * 2);
  st.vg = (const char*)(prob ? vB : vA) + (size_t)(b * NH + h) * (SK * DH * 2);
  st.ldsw = wave * 4096;
  st.rot = (h + 5 * b) & 15;
#pragma unroll
  for (int j = 0; j < 4; ++j) {
    const int row = wave * 16 + j * 4 + g;
    const int fK = row & 15;
    const int fV = ((row & 3) << 2) | ((((row >> 2) ^ (row >> 3)) & 1) << 1);
    st.offK[j] = row * 256 + ((r16 ^ fK) << 4);
    st.offV[j] = row * 256 + ((r16 ^ fV) << 4);
  }
  const int qq = r16 >> 2, pp = r16 & 3;
  const unsigned pb = ((pp >> 1) << 4) + ((pp & 1) << 3);
  st.aK  = r32 * 256 + ((hh ^ (r32 & 15)) << 4);
  st.aK8 = (wave * 16 + r16) * 256 + ((g ^ r16) << 4);
  st.aV  = (4 * hh + qq) * 256 + (qq << 6) + ((blk ^ hh) << 5) + pb;
  st.aV8 = (wave * 16 + 4 * g + qq) * 256 + (qq << 6) + ((((g & 1) ^ (g >> 1))) << 5) + pb;
  const unsigned aW  = (8 * hh + qq) * 256 + (qq << 6) + ((blk ^ hh) << 5) + pb;
  const unsigned aW8 = ((8 * g + qq) * 256 + (qq << 6) + ((g & 1) << 5) + pb) ^ ((unsigned)wave << 6);
  const unsigned xoff8 = (128 + r16) * (DM * 2) + g * 16;
  unsigned offX[4];
#pragma unroll
  for (int j = 0; j < 4; ++j) {
    const int row = wave * 32 + j * 8 + (lane >> 3);
    offX[j] = row * (DM * 2) + (((lane & 7) ^ ((row >> 1) & 7)) << 4);
  }
  const unsigned aX = (wave * 32 + r32) * 128 + ((hh ^ ((r32 >> 1) & 7)) << 4);

  s16x8 xf8[3][2];
  u32x4 wr[4][4];
  unsigned offWg[4], aWw;
#pragma unroll
  for (int j = 0; j < 4; ++j) offWg[j] = (wave * 16 + j * 4 + g) * (HD * 2) + (r16 << 4);
  aWw = wave * 4096 + g * 256;
  typedef __attribute__((address_space(3))) u32x4* lds_u32x4_ptr;
#define X_LD(DST, OFF, IMM) asm volatile("global_load_dwordx4 %0, %1, %2 offset:%3" : "=v"(DST) : "v"(OFF), "s"(xg), "i"(IMM) : "memory")
#define X8_LOAD(T) do { X_LD(xf8[(T) % 3][0], xoff8, (T) * 128); X_LD(xf8[(T) % 3][1], xoff8, (T) * 128 + 64); } while (0)
#define XD_ISSUE(T) do { _Pragma("unroll") for (int i_ = 0; i_ < 4; ++i_) dma16(((T) % 3) * K1_TILE + st.ldsw + i_ * 1024, offX[i_], xg + (T) * 128); } while (0)
#define W_LD(T) do { const char* gs_ = wg + (size_t)(T) * 64 * (HD * 2); _Pragma("unroll") for (int i_ = 0; i_ < 4; ++i_) \
    asm volatile("global_load_dwordx4 %0, %1, %2" : "=v"(wr[(T) & 3][i_]) : "v"(offWg[i_]), "s"(gs_) : "memory"); } while (0)
#define W_PIN(T) asm volatile("" : "+v"(wr[(T) & 3][0]), "+v"(wr[(T) & 3][1]), "+v"(wr[(T) & 3][2]), "+v"(wr[(T) & 3][3]))
#define W_WRITE(T) do { _Pragma("unroll") for (int i_ = 0; i_ < 4; ++i_) \
    *(lds_u32x4_ptr)(uintptr_t)((3 + ((T) & 1)) * K1_TILE + aWw + i_ * 1024 + ((r16 ^ ((g << 2) | (((i_ & 1) ^ (i_ >> 1)) << 1))) << 4)) = wr[(T) & 3][i_]; } while (0)

  u32x4 wop[10];
#pragma unroll
  for (int i = 0; i < 10; ++i) wop[i] = u32x4{0u, 0u, 0u, 0u};
  if (bid >= 256) {
    const int c = bid - 256;
    const char* wo = (const char*)((c & 1) ? woB : woA) + (size_t)(c >> 1) * 40960;
#pragma unroll
    for (int i = 0; i < 10; ++i) asm volatile("global_load_dwordx4 %0, %1, %2" : "+v"(wop[i]) : "v"((unsigned)(tid * 16)), "s"(wo + i * 4096) : "memory");
  }
  W_LD(0); W_LD(1);
  W_LD(2); XD_ISSUE(0); X8_LOAD(0);
  W_LD(3); XD_ISSUE(1); X8_LOAD(1);

  f32x16 qa[4]; f32x4 qa8[2];
#pragma unroll
  for (int dt = 0; dt < 4; ++dt)
#pragma unroll
    for (int r = 0; r < 16; ++r) qa[dt][r] = 0.f;
  qa8[0] = f32x4{0.f, 0.f, 0.f, 0.f}; qa8[1] = f32x4{0.f, 0.f, 0.f, 0.f};

  wait_vm<24>();
  asm volatile("" : "+v"(wop[0]), "+v"(wop[1]), "+v"(wop[2]), "+v"(wop[3]), "+v"(wop[4]), "+v"(wop[5]), "+v"(wop[6]), "+v"(wop[7]), "+v"(wop[8]), "+v"(wop[9]));
  W_PIN(0); W_WRITE(0);
#pragma unroll
  for (int t = 0; t < K1_NWS; ++t) {
    if (t <= 6) wait_vm<10>(); else if (t <= 8) wait_vm<6>(); else wait_vm<4>();
    asm volatile("" : "+v"(xf8[t % 3][0]), "+v"(xf8[t % 3][1]));
    if (t + 1 < K1_NWS) W_PIN(t + 1);
    bar();
    STAMP(5);
    if (t + 1 < K1_NWS) W_WRITE(t + 1);
    unsigned a0 = aW + (3 + (t & 1)) * K1_TILE, a8 = aW8 + (3 + (t & 1)) * K1_TILE, ax = aX + (t % 3) * K1_TILE;
    asm volatile("" : "+v"(a0), "+v"(a8), "+v"(ax));
    {
      s16x8 wf[2][4], xf[4];
#pragma unroll
      for (int kk = 0; kk < 4; ++kk) xf[kk] = lds_b128(ax ^ (kk << 5));
#pragma unroll
      for (int i = 0; i < 4; ++i) { const unsigned a = a0 ^ (i << 6); wf[0][i] = lds_tr2(a, (a ^ 32u) + 1024); }
#pragma unroll
      for (int kk = 0; kk < 4; ++kk) {
        if (kk < 3) {
#pragma unroll
          for (int i = 0; i < 4; ++i) { const unsigned a = (a0 ^ (i << 6)) + (kk + 1) * 4096; wf[(kk + 1) & 1][i] = lds_tr2(a, (a ^ 32u) + 1024); }
        } else {
#pragma unroll
          for (int i = 0; i < 4; ++i) { const unsigned a = (a8 ^ ((i & 1) << 5)) + (i >> 1) * 8192; wf[0][i] = lds_tr2(a, (a ^ 32u) + 1024); }
        }
#pragma unroll
        for (int i = 0; i < 4; ++i) qa[i] = mfma32(wf[kk & 1][i], xf[kk], qa[i]);
        __builtin_amdgcn_sched_barrier(0);
        if (kk == 0 && t + 4 < K1_NWS) W_LD(t + 4);
        if (kk == 1) { if (t + 2 < K1_NWS) XD_ISSUE(t + 2); else k1_issue_kv(st, t - 8, t == 8 ? 1 : 2); }
        if (kk == 2) { if (t + 2 < K1_NWS) X8_LOAD(t + 2); if (t == 9) k1_issue_kv(st, 2, 3); }
        __builtin_amdgcn_sched_barrier(0);
      }
#pragma unroll
      for (int i = 0; i < 4; ++i) qa8[i & 1] = mfma16(wf[0][i], xf8[t % 3][i >> 1], qa8[i & 1]);
    }
    STAMP(6);
  }
  {
#pragma unroll
    for (int s = 0; s < 8; ++s) {
      const f32x16& a = qa[s >> 1]; const int b0 = 8 * (s & 1);
      u32x4 w = pack_swap(a[b0] * SC_L2E, a[b0 + 1] * SC_L2E, a[b0 + 2] * SC_L2E, a[b0 + 3] * SC_L2E,
                          a[b0 + 4] * SC_L2E, a[b0 + 5] * SC_L2E, a[b0 + 6] * SC_L2E, a[b0 + 7] * SC_L2E);
      st.q[s] = __builtin_bit_cast(s16x8, w);
    }
    const int srcA = (r16 + 16 * (2 * (g & 1))) << 2, srcB = srcA + 64;
    const bool hiT = (g >> 1) != 0;
    s16x8 q8w = k1_qfrag16(qa8[0] * SC_L2E, qa8[1] * SC_L2E, srcA, srcB, hiT);
    const unsigned sc = 0;
    asm volatile("s_waitcnt lgkmcnt(0)" ::: "memory"); bar();
    *(__attribute__((address_space(3))) s16x8*)(uintptr_t)(sc + wave * 1024 + lane * 16) = q8w;
    asm volatile("s_waitcnt lgkmcnt(0)" ::: "memory"); bar();
#pragma unroll
    for (int s = 0; s < 4; ++s) st.q8[s] = lds_b128(sc + s * 1024 + lane * 16);
    asm volatile("s_waitcnt lgkmcnt(0)" ::: "memory");
  }
#pragma unroll
  for (int dt = 0; dt < 4; ++dt)
#pragma unroll
    for (int r = 0; r < 16; ++r) st.o[dt][r] = 0.f;
#pragma unroll
  for (int dt = 0; dt < 8; ++dt) st.o8[dt] = f32x4{0.f, 0.f, 0.f, 0.f};
#pragma unroll
  for (int r = 0; r < 16; ++r) st.negm[r] = 0.f;
  st.negm8 = f32x4{0.f, 0.f, 0.f, 0.f};
  st.m = st.m8 = 0.f; st.l = st.l8 = 0.f;

  int slot = 0;
#pragma unroll 1
  for (int it = 0; it < 16; ++it) k1_pair(st, it, slot);

  int tz = threadIdx.x; asm volatile("" : "+v"(tz));
  char* ob = (char*)ows + ((size_t)(prob * MROWS + b * SQ) * HD + h * DH) * 2;
  {
    const float inv = 1.f / xsum32(st.l);
    char* og = ob + (size_t)(wave * 32 + (tz & 31)) * (HD * 2) + ((tz >> 5) & 1) * 16;
#pragma unroll
    for (int dt = 0; dt < 4; ++dt)
#pragma unroll
      for (int s2 = 0; s2 < 2; ++s2) {
        const f32x16& a = st.o[dt]; const int b0 = 8 * s2;
        u32x4 w = pack_swap(a[b0] * inv, a[b0 + 1] * inv, a[b0 + 2] * inv, a[b0 + 3] * inv, a[b0 + 4] * inv, a[b0 + 5] * inv, a[b0 + 6] * inv, a[b0 + 7] * inv);
        *(u32x4*)(og + dt * 64 + s2 * 32) = w;
      }
  }
  {
    typedef __attribute__((address_space(3))) f32x4* lds_f32x4_ptr;
    typedef __attribute__((address_space(3))) float* lds_f32_ptr;
    const int l2 = tz & 63, r = tz & 15;
    const float l8t = xsum16_32(st.l8);
    asm volatile("s_waitcnt lgkmcnt(0)" ::: "memory"); bar();
#pragma unroll
    for (int dt = 0; dt < 8; ++dt) *(lds_f32x4_ptr)(uintptr_t)(wave * 8192 + dt * 1024 + l2 * 16) = st.o8[dt];
    if (l2 < 16) { *(lds_f32_ptr)(uintptr_t)(32768 + (wave * 16 + r) * 4) = st.m8; *(lds_f32_ptr)(uintptr_t)(32768 + 256 + (wave * 16 + r) * 4) = l8t; }
    asm volatile("s_waitcnt lgkmcnt(0)" ::: "memory"); bar();
    float mw[4], lw[4];
#pragma unroll
    for (int w = 0; w < 4; ++w) { mw[w] = *(lds_f32_ptr)(uintptr_t)(32768 + (w * 16 + r) * 4); lw[w] = *(lds_f32_ptr)(uintptr_t)(32768 + 256 + (w * 16 + r) * 4); }
    const float M = fmaxf(fmaxf(mw[0], mw[1]), fmaxf(mw[2], mw[3]));
    float L = 0.f, sc[4];
#pragma unroll
    for (int w = 0; w < 4; ++w) { sc[w] = __builtin_amdgcn_exp2f(mw[w] - M); L += lw[w] * sc[w]; }
    const float inv = 1.f / L;
    char* og = ob + (size_t)(128 + r) * (HD * 2) + ((l2 >> 4) & 3) * 8;
#pragma unroll
    for (int d2 = 0; d2 < 2; ++d2) {
      const int dt = 2 * wave + d2;
      f32x4 acc = {0.f, 0.f, 0.f, 0.f};
#pragma unroll
      for (int w = 0; w < 4; ++w) { f32x4 v = *(lds_f32x4_ptr)(uintptr_t)(w * 8192 + dt * 1024 + l2 * 16); acc += v * sc[w]; }
      u32x2 wv = {cvtpk(acc[0] * inv, acc[1] * inv), cvtpk(acc[2] * inv, acc[3] * inv)};
      *(u32x2*)(og + dt * 32) = wv;
    }
  }
}

constexpr int K2_BM = 96, K2_BN = 64, K2_BK = 128;
constexpr int K2_OT = K2_BM * K2_BK * 2;
constexpr int K2_WT = K2_BK * K2_BN * 2;
constexpr int K2_ST = K2_OT + K2_WT;
#define K2NS 4
constexpr int K2_NS = K2NS;
constexpr int K2_LDS = K2_NS * K2_ST;
constexpr int K2_STEPS = HD / K2_BK;
constexpr int K2_NLD = 4;
constexpr int K2_THREADS = (8 + K2_NLD) * 64;

__global__ __launch_bounds__(K2_THREADS, 3)
void mha_oproj_kernel(const __hip_bfloat16* __restrict__ ows, const __hip_bfloat16* __restrict__ woA,
                      const __hip_bfloat16* __restrict__ woB, __hip_bfloat16* __restrict__ out) {
  const int tid = threadIdx.x;
  const int wave = __builtin_amdgcn_readfirstlane(tid >> 6);
  const int lane = tid & 63;
  const int r16 = lane & 15, g = lane >> 4;
  const int bid = blockIdx.x;
  const int xcd = bid & 7, loc = bid >> 3;
  const int mblk = xcd * 3 + loc / 10, nblk = loc % 10;
  const int prob = mblk / 12, mb = mblk % 12;
  if (wave >= 8) {
    const int lw = wave - 8;
    const char* og = (const char*)ows + (size_t)(prob * MROWS + mb * K2_BM) * (HD * 2);
    const char* wg = (const char*)(prob ? woB : woA) + (size_t)nblk * (K2_BN * 2);
    const int rot = 0;
    unsigned offO[6], offWo[4];
#pragma unroll
    for (int j = 0; j < 6; ++j) { const int row = lw * 24 + j * 4 + g; offO[j] = row * (HD * 2) + ((r16 ^ (row & 15)) << 4); }
#pragma unroll
    for (int j = 0; j < 4; ++j) {
      const int row = lw * 32 + j * 8 + (lane >> 3);
      offWo[j] = row * (DM * 2) + (((lane & 7) ^ (((row >> 1) & 1) << 2)) << 4);
    }
#define K2_DMA_O dma16
#define K2_DMA_W dma16
#define K2_ISSUE(T, SL) do { const char* go = og + (size_t)(T) * (K2_BK * 2); const char* gw = wg + (size_t)(T) * K2_BK * (DM * 2); \
    const unsigned lb = (SL) * K2_ST; \
    _Pragma("unroll") for (int j_ = 0; j_ < 6; ++j_) K2_DMA_O(lb + lw * 6144 + j_ * 1024, offO[j_], go); \
    _Pragma("unroll") for (int j_ = 0; j_ < 4; ++j_) K2_DMA_W(lb + K2_OT + lw * 4096 + j_ * 1024, offWo[j_], gw); } while (0)
#pragma unroll
    for (int i = 0; i < K2_NS - 1; ++i) K2_ISSUE((i + rot) & 31, i);
    int slot = 0;
#pragma unroll 1
    for (int t = 0; t < K2_STEPS; ++t) {
      if (t + K2_NS - 2 < K2_STEPS) wait_vm<10 * (K2_NS - 2)>();
      else if (K2_NS > 3 && t + K2_NS - 3 < K2_STEPS) wait_vm<(K2_NS > 3 ? 10 * (K2_NS - 3) : 0)>();
      else wait_vm<0>();
      bar();
      if (t + K2_NS - 1 < K2_STEPS) { int s2 = slot + K2_NS - 1; if (s2 >= K2_NS) s2 -= K2_NS; K2_ISSUE((t + K2_NS - 1 + rot) & 31, s2); }
      slot = (slot + 1 == K2_NS) ? 0 : slot + 1;
    }
    bar(); bar();
    return;
  }
  const int kq = wave >> 1, wn = wave & 1;
  const int r32 = lane & 31, hh = lane >> 5, blk = g & 1, qq = r16 >> 2, pp = r16 & 3;
  const unsigned aO = r32 * 256 + (((4 * kq + hh) ^ (r32 & 15)) << 4);
  unsigned aWo;
  {
    const int rowl = 8 * hh + qq;
    aWo = K2_OT + (32 * kq + rowl) * 128 + (((4 * wn + 2 * blk + (pp >> 1)) ^ (((rowl >> 1) & 1) << 2)) << 4) + ((pp & 1) << 3);
  }
  f32x16 acc[3];
#pragma unroll
  for (int i = 0; i < 3; ++i)
#pragma unroll
    for (int r = 0; r < 16; ++r) acc[i][r] = 0.f;
  int slot = 0;
  s16x8 wfA[2], ofA[2][3], wfB[2], ofB[2][3];
#define K2_READ(WF, OF) do { const unsigned sb = slot * K2_ST; \
    _Pragma("unroll") for (int s2 = 0; s2 < 2; ++s2) { const unsigned aw = aWo + sb + s2 * 16 * 128; WF[s2] = lds_tr2(aw, aw + 512); \
      const unsigned ao = (aO ^ (s2 << 5)) + sb; _Pragma("unroll") for (int mt = 0; mt < 3; ++mt) OF[s2][mt] = lds_b128(ao + mt * 8192); } \
    slot = (slot + 1 == K2_NS) ? 0 : slot + 1; } while (0)
#define K2_MMA(WF, OF) do { _Pragma("unroll") for (int s2 = 0; s2 < 2; ++s2) _Pragma("unroll") for (int mt = 0; mt < 3; ++mt) acc[mt] = mfma32(WF[s2], OF[s2][mt], acc[mt]); } while (0)
  bar(); K2_READ(wfA, ofA);
#pragma unroll 1
  for (int t = 1; t < K2_STEPS - 1; t += 2) {
    bar(); K2_READ(wfB, ofB); __builtin_amdgcn_sched_barrier(0); K2_MMA(wfA, ofA); __builtin_amdgcn_sched_barrier(0);
    bar(); K2_READ(wfA, ofA); __builtin_amdgcn_sched_barrier(0); K2_MMA(wfB, ofB); __builtin_amdgcn_sched_barrier(0);
  }
  bar(); K2_READ(wfB, ofB); __builtin_amdgcn_sched_barrier(0); K2_MMA(wfA, ofA); __builtin_amdgcn_sched_barrier(0);
  K2_MMA(wfB, ofB);
  {
    typedef __attribute__((address_space(3))) f32x4* lds_f32x4_ptr;
    bar();
    if (kq > 0) {
#pragma unroll
      for (int mt = 0; mt < 3; ++mt)
#pragma unroll
        for (int q4 = 0; q4 < 4; ++q4)
          *(lds_f32x4_ptr)(uintptr_t)((((kq - 1) * 2 + wn) * 12 + mt * 4 + q4) * 1024 + lane * 16) = f32x4{acc[mt][4 * q4], acc[mt][4 * q4 + 1], acc[mt][4 * q4 + 2], acc[mt][4 * q4 + 3]};
    }
    bar();
    if (kq == 0) {
#pragma unroll
      for (int mt = 0; mt < 3; ++mt) {
#pragma unroll
        for (int q4 = 0; q4 < 4; ++q4)
#pragma unroll
          for (int k2 = 0; k2 < 3; ++k2) {
            f32x4 v = *(lds_f32x4_ptr)(uintptr_t)(((k2 * 2 + wn) * 12 + mt * 4 + q4) * 1024 + lane * 16);
            acc[mt][4 * q4] += v[0]; acc[mt][4 * q4 + 1] += v[1]; acc[mt][4 * q4 + 2] += v[2]; acc[mt][4 * q4 + 3] += v[3];
          }
        const int row = mb * K2_BM + mt * 32 + r32;
        char* op = (char*)out + ((size_t)row * (2 * DM) + prob * DM + nblk * K2_BN + wn * 32 + hh * 8) * 2;
#pragma unroll
        for (int s2 = 0; s2 < 2; ++s2) {
          const int b0 = 8 * s2;
          u32x4 w = pack_swap(acc[mt][b0], acc[mt][b0 + 1], acc[mt][b0 + 2], acc[mt][b0 + 3], acc[mt][b0 + 4], acc[mt][b0 + 5], acc[mt][b0 + 6], acc[mt][b0 + 7]);
          *(u32x4*)(op + s2 * 32) = w;
        }
      }
    }
  }
}

extern "C" void kernel_launch(void* const* d_in, const int* in_sizes, int n_in,
                              void* d_out, int out_size, void* d_ws, size_t ws_size,
                              hipStream_t stream) {
  const __hip_bfloat16* xA  = (const __hip_bfloat16*)d_in[0];
  const __hip_bfloat16* kA  = (const __hip_bfloat16*)d_in[1];
  const __hip_bfloat16* vA  = (const __hip_bfloat16*)d_in[2];
  const __hip_bfloat16* wqA = (const __hip_bfloat16*)d_in[3];
  const __hip_bfloat16* woA = (const __hip_bfloat16*)d_in[4];
  const __hip_bfloat16* xB  = (const __hip_bfloat16*)d_in[5];
  const __hip_bfloat16* kB  = (const __hip_bfloat16*)d_in[6];
  const __hip_bfloat16* vB  = (const __hip_bfloat16*)d_in[7];
  const __hip_bfloat16* wqB = (const __hip_bfloat16*)d_in[8];
  const __hip_bfloat16* woB = (const __hip_bfloat16*)d_in[9];
  __hip_bfloat16* ows = (__hip_bfloat16*)d_ws;
  (void)hipFuncSetAttribute((const void*)mha_attn_kernel, hipFuncAttributeMaxDynamicSharedMemorySize, K1_LDS);
  (void)hipFuncSetAttribute((const void*)mha_oproj_kernel, hipFuncAttributeMaxDynamicSharedMemorySize, K2_LDS);
  hipLaunchKernelGGL(mha_attn_kernel, dim3(512), dim3(K1_WAVES * 64), K1_LDS, stream, xA, kA, vA, wqA, xB, kB, vB, wqB, woA, woB, ows);
  hipLaunchKernelGGL(mha_oproj_kernel, dim3(240), dim3(K2_THREADS), K2_LDS, stream, (const __hip_bfloat16*)ows, woA, woB, (__hip_bfloat16*)d_out);
}
```

```cpp
#include <hip/hip_runtime.h>
#include <hip/hip_bf16.h>
#include <stdint.h>

typedef short    s16x4 __attribute__((ext_vector_type(4)));
typedef short    s16x8 __attribute__((ext_vector_type(8)));
typedef float    f32x4 __attribute__((ext_vector_type(4)));
typedef float    f32x2 __attribute__((ext_vector_type(2)));
typedef __bf16   bf16x2 __attribute__((ext_vector_type(2)));
typedef unsigned u32x4 __attribute__((ext_vector_type(4)));
typedef unsigned u32x2 __attribute__((ext_vector_type(2)));
typedef __attribute__((address_space(3))) s16x4* lds_s16x4_ptr;
typedef __attribute__((address_space(3))) const s16x8* lds_s16x8_ptr;
#define DI __device__ __forceinline__

constexpr int NB = 8, SQ = 144, DM = 640, NH = 32, SK = 1024, DH = 128;
constexpr int HD = NH * DH;
constexpr int MROWS = NB * SQ;

DI unsigned cvtpk(float lo, float hi) {
  f32x2 v = {lo, hi};
  bf16x2 b = __builtin_convertvector(v, bf16x2);
  return __builtin_bit_cast(unsigned, b);
}
DI f32x4 mfma16(s16x8 a, s16x8 b, f32x4 c) {
  return __builtin_amdgcn_mfma_f32_16x16x32_bf16(a, b, c, 0, 0, 0);
}
DI void dma16(unsigned lds_addr, unsigned voff, const char* gbase) {
  asm volatile("s_mov_b32 m0, %0\n\ts_nop 0\n\tglobal_load_lds_dwordx4 %1, %2"
               :: "s"(lds_addr), "v"(voff), "s"(gbase) : "memory");
}
DI void dma16nt(unsigned lds_addr, unsigned voff, const char* gbase) {
  asm volatile("s_mov_b32 m0, %0\n\ts_nop 0\n\tglobal_load_lds_dwordx4 %1, %2 nt"
               :: "s"(lds_addr), "v"(voff), "s"(gbase) : "memory");
}
template <int N> DI void wait_vm() { asm volatile("s_waitcnt vmcnt(%0)" :: "i"(N) : "memory"); }
DI void bar() { asm volatile("s_waitcnt lgkmcnt(0)\n\ts_barrier" ::: "memory"); }
DI s16x8 lds_b128(unsigned addr) { return *(lds_s16x8_ptr)(uintptr_t)addr; }
DI s16x8 lds_tr2(unsigned addr0, unsigned addr1) {
  s16x4 lo = __builtin_amdgcn_ds_read_tr16_b64_v4i16((lds_s16x4_ptr)(uintptr_t)addr0);
  s16x4 hi = __builtin_amdgcn_ds_read_tr16_b64_v4i16((lds_s16x4_ptr)(uintptr_t)addr1);
  return __builtin_shufflevector(lo, hi, 0, 1, 2, 3, 4, 5, 6, 7);
}
DI float xmax16_32(float v) {
  auto r = __builtin_amdgcn_permlane32_swap(__float_as_uint(v), __float_as_uint(v), false, false);
  v = fmaxf(__uint_as_float(r[0]), __uint_as_float(r[1]));
  auto r2 = __builtin_amdgcn_permlane16_swap(__float_as_uint(v), __float_as_uint(v), false, false);
  return fmaxf(__uint_as_float(r2[0]), __uint_as_float(r2[1]));
}
DI float xsum16_32(float v) {
  auto r = __builtin_amdgcn_permlane32_swap(__float_as_uint(v), __float_as_uint(v), false, false);
  v = __uint_as_float(r[0]) + __uint_as_float(r[1]);
  auto r2 = __builtin_amdgcn_permlane16_swap(__float_as_uint(v), __float_as_uint(v), false, false);
  return __uint_as_float(r2[0]) + __uint_as_float(r2[1]);
}

typedef float f32x16 __attribute__((ext_vector_type(16)));
constexpr int K1_WAVES = 4;
constexpr int K1_NSLOT = 5;
constexpr int K1_TILE  = 16384;
constexpr int K1_NWS   = 10;
constexpr int K1_NW    = 20;
constexpr int K1_LDS   = K1_NSLOT * K1_TILE;
constexpr float SC_L2E = 0.088388347648318447f * 1.4426950408889634f;
constexpr float K1_THR = 8.0f;

DI f32x4 mfma16k16(s16x4 a, s16x4 b, f32x4 c) { return __builtin_amdgcn_mfma_f32_16x16x16bf16_1k(a, b, c, 0, 0, 0); }
DI f32x16 mfma32(s16x8 a, s16x8 b, f32x16 c) { return __builtin_amdgcn_mfma_f32_32x32x16_bf16(a, b, c, 0, 0, 0); }
DI s16x4 lds_tr(unsigned addr) { return __builtin_amdgcn_ds_read_tr16_b64_v4i16((lds_s16x4_ptr)(uintptr_t)addr); }
DI float max3f(float a, float b, float c) { float r; asm("v_max3_f32 %0, %1, %2, %3" : "=v"(r) : "v"(a), "v"(b), "v"(c)); return r; }
DI float max2f(float a, float b) { float r; asm("v_max_f32_e32 %0, %1, %2" : "=v"(r) : "v"(a), "v"(b)); return r; }
DI float xmax32(float v) {
  auto r = __builtin_amdgcn_permlane32_swap(__float_as_uint(v), __float_as_uint(v), false, false);
  return max2f(__uint_as_float(r[0]), __uint_as_float(r[1]));
}
DI float xmax16(float v) {
  auto r = __builtin_amdgcn_permlane16_swap(__float_as_uint(v), __float_as_uint(v), false, false);
  return max2f(__uint_as_float(r[0]), __uint_as_float(r[1]));
}
DI float xsum32(float v) {
  auto r = __builtin_amdgcn_permlane32_swap(__float_as_uint(v), __float_as_uint(v), false, false);
  return __uint_as_float(r[0]) + __uint_as_float(r[1]);
}
DI u32x4 pack_swap(float a0, float a1, float a2, float a3, float b0, float b1, float b2, float b3) {
  unsigned A0 = cvtpk(a0, a1), A1 = cvtpk(a2, a3), B0 = cvtpk(b0, b1), B1 = cvtpk(b2, b3);
  auto r0 = __builtin_amdgcn_permlane32_swap(A0, B0, false, false);
  auto r1 = __builtin_amdgcn_permlane32_swap(A1, B1, false, false);
  return u32x4{r0[0], r1[0], r0[1], r1[1]};
}

#define STAMP(i) do {} while (0)
struct K1State {
  unsigned offK, offV;
  const char *kg, *vg;
  unsigned ldsw;
  unsigned aK, aV, aK8, aV8;
  int rot, wave;
  s16x8 q[8], q8[4];
  f32x16 o[4]; f32x4 o8[8];
  f32x16 negm; f32x4 negm8;
  float m, l, m8, l8;
  s16x8 p[4];
  s16x4 p8;
};

DI int k1_phys(int lg) { return lg; }
DI void k1_issue_piece(K1State& st, int n, int slot, int i) {
  const int t = ((n >> 1) + st.rot) & 15;
  const unsigned lb = slot * K1_TILE + st.ldsw + i * 1024;
  if (n & 1) dma16nt(lb, st.offV ^ ((((i & 1) ^ (i >> 1))) << 5), st.vg + (size_t)t * K1_TILE + i * 1024);
  else       dma16nt(lb, st.offK ^ (i << 6), st.kg + (size_t)t * K1_TILE + i * 1024);
}
DI void k1_issue_kv(K1State& st, int n, int slot) {
#pragma unroll
  for (int i = 0; i < 4; ++i) k1_issue_piece(st, n, slot, i);
}

DI void k1_kstep(K1State& st, int slot, bool FIRST, bool ISSUE, int jn, int sn) {
  unsigned a0 = st.aK + slot * K1_TILE, a8 = st.aK8 + slot * K1_TILE;
  asm volatile("" : "+v"(a0), "+v"(a8));
  f32x16 s[2]; f32x4 s8;
  s16x8 kf[3][2], k8[2];
#pragma unroll
  for (int i = 0; i < 2; ++i) { kf[i][0] = lds_b128(a0 ^ (i << 5)); kf[i][1] = lds_b128((a0 ^ (i << 5)) + 8192); }
  k8[0] = lds_b128(a8);
#pragma unroll
  for (int sg = 0; sg < 8; ++sg) {
    if (sg < 6) { kf[(sg + 2) % 3][0] = lds_b128(a0 ^ ((sg + 2) << 5)); kf[(sg + 2) % 3][1] = lds_b128((a0 ^ ((sg + 2) << 5)) + 8192); }
    if ((sg & 1) == 0 && sg < 6) k8[((sg >> 1) + 1) & 1] = lds_b128(a8 ^ (((sg >> 1) + 1) << 6));
    if (sg == 0) {
      s[0] = mfma32(kf[0][0], st.q[0], st.negm);
      s[1] = mfma32(kf[0][1], st.q[0], st.negm);
      s8 = mfma16(k8[0], st.q8[0], st.negm8);
    } else {
      s[0] = mfma32(kf[sg % 3][0], st.q[sg], s[0]);
      s[1] = mfma32(kf[sg % 3][1], st.q[sg], s[1]);
      if ((sg & 1) == 0) s8 = mfma16(k8[(sg >> 1) & 1], st.q8[sg >> 1], s8);
    }
    __builtin_amdgcn_sched_barrier(0);
    if (ISSUE && st.wave == (sg >> 1)) { k1_issue_piece(st, jn, sn, 2 * (sg & 1)); k1_issue_piece(st, jn, sn, 2 * (sg & 1) + 1); }
    __builtin_amdgcn_sched_barrier(0);
  }
  STAMP(1);
  float t0 = max3f(s[0][0], s[0][1], s[0][2]), t1 = max3f(s[1][0], s[1][1], s[1][2]);
#pragma unroll
  for (int r = 3; r < 15; r += 2) { t0 = max3f(t0, s[0][r], s[0][r + 1]); t1 = max3f(t1, s[1][r], s[1][r + 1]); }
  float tm = max3f(t0, t1, max2f(s[0][15], s[1][15]));
  tm = xmax32(tm);
  float t8 = max2f(max2f(s8[0], s8[1]), max2f(s8[2], s8[3]));
  t8 = xmax16(xmax32(t8));
  if (FIRST || __any((tm > K1_THR) | (t8 > K1_THR))) {
    const float dl = FIRST ? tm : max2f(tm, 0.f);
    const float d8 = FIRST ? t8 : max2f(t8, 0.f);
    st.m += dl; st.m8 += d8;
#pragma unroll
    for (int r = 0; r < 16; ++r) { s[0][r] -= dl; s[1][r] -= dl; st.negm[r] = -st.m; }
#pragma unroll
    for (int r = 0; r < 4; ++r) { s8[r] -= d8; st.negm8[r] = -st.m8; }
    {
      const float f = FIRST ? 0.f : __builtin_amdgcn_exp2f(-dl), f8 = FIRST ? 0.f : __builtin_amdgcn_exp2f(-d8);
      st.l *= f; st.l8 *= f8;
#pragma unroll
      for (int dt = 0; dt < 4; ++dt)
#pragma unroll
        for (int r = 0; r < 16; ++r) st.o[dt][r] *= f;
#pragma unroll
      for (int dt = 0; dt < 8; ++dt)
#pragma unroll
        for (int r = 0; r < 4; ++r) st.o8[dt][r] *= f8;
    }
  }
  float ps0 = 0.f, ps1 = 0.f, ps8 = 0.f;
#pragma unroll
  for (int r = 0; r < 16; ++r) {
    float e0 = __builtin_amdgcn_exp2f(s[0][r]), e1 = __builtin_amdgcn_exp2f(s[1][r]);
    s[0][r] = e0; s[1][r] = e1; ps0 += e0; ps1 += e1;
  }
#pragma unroll
  for (int r = 0; r < 4; ++r) { float e = __builtin_amdgcn_exp2f(s8[r]); s8[r] = e; ps8 += e; }
  st.l += ps0 + ps1; st.l8 += ps8;
#pragma unroll
  for (int ks = 0; ks < 4; ++ks) {
    const int kt = ks >> 1, b = 8 * (ks & 1);
    u32x4 w = {cvtpk(s[kt][b], s[kt][b + 1]), cvtpk(s[kt][b + 2], s[kt][b + 3]), cvtpk(s[kt][b + 4], s[kt][b + 5]), cvtpk(s[kt][b + 6], s[kt][b + 7])};
    st.p[ks] = __builtin_bit_cast(s16x8, w);
  }
  u32x2 w8 = {cvtpk(s8[0], s8[1]), cvtpk(s8[2], s8[3])};
  st.p8 = __builtin_bit_cast(s16x4, w8);
}

DI void k1_vstep(K1State& st, int slot, bool ISSUE, int jn, int sn) {
  unsigned a0 = st.aV + slot * K1_TILE, a8 = st.aV8 + slot * K1_TILE;
  asm volatile("" : "+v"(a0), "+v"(a8));
  s16x8 vf[2][4];
#pragma unroll
  for (int i = 0; i < 4; ++i) { const unsigned a = a0 ^ (i << 6); vf[0][i] = lds_tr2(a, (a ^ 32u) + 2048); }
#pragma unroll
  for (int ks = 0; ks < 4; ++ks) {
    if (ks < 3) {
#pragma unroll
      for (int i = 0; i < 4; ++i) { const unsigned a = (a0 ^ (i << 6)) + (ks + 1) * 4096; vf[(ks + 1) & 1][i] = lds_tr2(a, (a ^ 32u) + 2048); }
    } else {
#pragma unroll
      for (int i = 0; i < 4; ++i) vf[0][i] = lds_tr2(a8 ^ ((2 * i) << 5), a8 ^ ((2 * i + 1) << 5));
    }
#pragma unroll
    for (int i = 0; i < 4; ++i) st.o[i] = mfma32(vf[ks & 1][i], st.p[ks], st.o[i]);
    __builtin_amdgcn_sched_barrier(0);
    if (ISSUE && st.wave == ks) k1_issue_kv(st, jn, sn);
    __builtin_amdgcn_sched_barrier(0);
  }
#pragma unroll
  for (int i = 0; i < 4; ++i) {
    s16x4 lo = __builtin_shufflevector(vf[0][i], vf[0][i], 0, 1, 2, 3), hi = __builtin_shufflevector(vf[0][i], vf[0][i], 4, 5, 6, 7);
    st.o8[2 * i] = mfma16k16(lo, st.p8, st.o8[2 * i]);
    st.o8[2 * i + 1] = mfma16k16(hi, st.p8, st.o8[2 * i + 1]);
  }
}

DI void k1_pair(K1State& st, int it, int& lg) {
  const int n = 2 * it;
  if (it < 15) wait_vm<12>(); else wait_vm<4>();
  bar();
  STAMP(0);
  int l4 = lg + 4; if (l4 >= 5) l4 -= 5;
  k1_kstep(st, k1_phys(lg), it == 0, it < 14, n + 4, k1_phys(l4));
  lg = (lg == 4) ? 0 : lg + 1;
  STAMP(2);
  if (it < 14) wait_vm<12>(); else if (it == 14) wait_vm<8>(); else wait_vm<0>();
  bar();
  STAMP(3);
  l4 = lg + 4; if (l4 >= 5) l4 -= 5;
  k1_vstep(st, k1_phys(lg), it < 14, n + 5, k1_phys(l4));
  lg = (lg == 4) ? 0 : lg + 1;
  STAMP(4);
}

DI s16x8 k1_qfrag16(const f32x4& t0, const f32x4& t1, int srcA, int srcB, bool hiT) {
  unsigned p00 = cvtpk(t0[0], t0[1]), p01 = cvtpk(t0[2], t0[3]), p10 = cvtpk(t1[0], t1[1]), p11 = cvtpk(t1[2], t1[3]);
  u32x4 w;
  { unsigned a0 = __builtin_amdgcn_ds_bpermute(srcA, p00), a1 = __builtin_amdgcn_ds_bpermute(srcA, p10); w[0] = hiT ? a1 : a0; }
  { unsigned a0 = __builtin_amdgcn_ds_bpermute(srcA, p01), a1 = __builtin_amdgcn_ds_bpermute(srcA, p11); w[1] = hiT ? a1 : a0; }
  { unsigned a0 = __builtin_amdgcn_ds_bpermute(srcB, p00), a1 = __builtin_amdgcn_ds_bpermute(srcB, p10); w[2] = hiT ? a1 : a0; }
  { unsigned a0 = __builtin_amdgcn_ds_bpermute(srcB, p01), a1 = __builtin_amdgcn_ds_bpermute(srcB, p11); w[3] = hiT ? a1 : a0; }
  return __builtin_bit_cast(s16x8, w);
}

__global__ __launch_bounds__(K1_WAVES * 64, 2)
void mha_attn_kernel(const __hip_bfloat16* __restrict__ kA, const __hip_bfloat16* __restrict__ vA,
                     const __hip_bfloat16* __restrict__ kB, const __hip_bfloat16* __restrict__ vB,
                     const __hip_bfloat16* __restrict__ woA, const __hip_bfloat16* __restrict__ woB,
                     const __hip_bfloat16* __restrict__ qws, __hip_bfloat16* __restrict__ ows) {
  const int tid = threadIdx.x;
  const int wave = __builtin_amdgcn_readfirstlane(tid >> 6);
  const int lane = tid & 63;
  const int r16 = lane & 15, g = lane >> 4, r32 = lane & 31, hh = lane >> 5, blk = g & 1;
  const int bid = blockIdx.x;
  const int prob = bid >> 8, b = (bid >> 5) & 7, h = bid & 31;
  K1State st;
  st.kg = (const char*)(prob ? kB : kA) + (size_t)(b * NH + h) * (SK * DH * 2);
  st.vg = (const char*)(prob ? vB : vA) + (size_t)(b * NH + h) * (SK * DH * 2);
  st.ldsw = wave * 4096; st.wave = wave;
  st.rot = (h + 5 * b) & 15;
  st.offK = (wave * 16 + g) * 256 + ((r16 ^ g) << 4);
  st.offV = (wave * 16 + g) * 256 + ((r16 ^ (g << 2)) << 4);
#pragma unroll
  for (int n = 0; n < 4; ++n) k1_issue_kv(st, n, n);
  const int qq = r16 >> 2, pp = r16 & 3;
  const unsigned pb = ((pp >> 1) << 4) + ((pp & 1) << 3);
  st.aK  = r32 * 256 + ((hh ^ (r32 & 15)) << 4);
  st.aK8 = (wave * 16 + r16) * 256 + ((g ^ r16) << 4);
  st.aV  = (4 * hh + qq) * 256 + (qq << 6) + ((blk ^ hh) << 5) + pb;
  st.aV8 = (wave * 16 + 4 * g + qq) * 256 + (qq << 6) + ((((g & 1) ^ (g >> 1))) << 5) + pb;
  {
    const char* qb = (const char*)qws + ((size_t)(prob * MROWS + b * SQ) * HD + h * DH) * 2;
    const char* qm = qb + (size_t)(wave * 32 + r32) * (HD * 2) + hh * 16;
    const char* q8p = qb + (size_t)(128 + r16) * (HD * 2) + g * 16;
#pragma unroll
    for (int s = 0; s < 8; ++s) st.q[s] = *(const s16x8*)(qm + s * 32);
#pragma unroll
    for (int s = 0; s < 4; ++s) st.q8[s] = *(const s16x8*)(q8p + s * 64);
    const char* wo = (const char*)((bid & 1) ? woB : woA) + (size_t)(bid >> 1) * 20480 + tid * 16;
    u32x4 wop[5];
#pragma unroll
    for (int i = 0; i < 5; ++i) wop[i] = *(const u32x4*)(wo + i * 4096);
#pragma unroll
    for (int i = 0; i < 5; ++i) asm volatile("" :: "v"(wop[i]));
  }
#pragma unroll
  for (int dt = 0; dt < 4; ++dt)
#pragma unroll
    for (int r = 0; r < 16; ++r) st.o[dt][r] = 0.f;
#pragma unroll
  for (int dt = 0; dt < 8; ++dt) st.o8[dt] = f32x4{0.f, 0.f, 0.f, 0.f};
#pragma unroll
  for (int r = 0; r < 16; ++r) st.negm[r] = 0.f;
  st.negm8 = f32x4{0.f, 0.f, 0.f, 0.f};
  st.m = st.m8 = 0.f; st.l = st.l8 = 0.f;

  int slot = 0;
#pragma unroll 1
  for (int it = 0; it < 16; ++it) k1_pair(st, it, slot);

  const int tz = __builtin_amdgcn_mbcnt_hi(~0u, __builtin_amdgcn_mbcnt_lo(~0u, 0u));
  char* ob = (char*)ows + ((size_t)(prob * MROWS + b * SQ) * HD + h * DH) * 2;
  {
    const float inv = 1.f / xsum32(st.l);
    char* og = ob + (size_t)(wave * 32 + (tz & 31)) * (HD * 2) + ((tz >> 5) & 1) * 16;
#pragma unroll
    for (int dt = 0; dt < 4; ++dt)
#pragma unroll
      for (int s2 = 0; s2 < 2; ++s2) {
        const f32x16& a = st.o[dt]; const int b0 = 8 * s2;
        u32x4 w = pack_swap(a[b0] * inv, a[b0 + 1] * inv, a[b0 + 2] * inv, a[b0 + 3] * inv, a[b0 + 4] * inv, a[b0 + 5] * inv, a[b0 + 6] * inv, a[b0 + 7] * inv);
        *(u32x4*)(og + dt * 64 + s2 * 32) = w;
      }
  }
  {
    typedef __attribute__((address_space(3))) f32x4* lds_f32x4_ptr;
    typedef __attribute__((address_space(3))) float* lds_f32_ptr;
    const int l2 = tz & 63, r = tz & 15;
    const float l8t = xsum16_32(st.l8);
    asm volatile("s_waitcnt lgkmcnt(0)" ::: "memory"); bar();
#pragma unroll
    for (int dt = 0; dt < 8; ++dt) *(lds_f32x4_ptr)(uintptr_t)(wave * 8192 + dt * 1024 + l2 * 16) = st.o8[dt];
    if (l2 < 16) { *(lds_f32_ptr)(uintptr_t)(32768 + (wave * 16 + r) * 4) = st.m8; *(lds_f32_ptr)(uintptr_t)(32768 + 256 + (wave * 16 + r) * 4) = l8t; }
    asm volatile("s_waitcnt lgkmcnt(0)" ::: "memory"); bar();
    float mw[4], lw[4];
#pragma unroll
    for (int w = 0; w < 4; ++w) { mw[w] = *(lds_f32_ptr)(uintptr_t)(32768 + (w * 16 + r) * 4); lw[w] = *(lds_f32_ptr)(uintptr_t)(32768 + 256 + (w * 16 + r) * 4); }
    const float M = fmaxf(fmaxf(mw[0], mw[1]), fmaxf(mw[2], mw[3]));
    float L = 0.f, sc[4];
#pragma unroll
    for (int w = 0; w < 4; ++w) { sc[w] = __builtin_amdgcn_exp2f(mw[w] - M); L += lw[w] * sc[w]; }
    const float inv = 1.f / L;
    char* og = ob + (size_t)(128 + r) * (HD * 2) + ((l2 >> 4) & 3) * 8;
#pragma unroll
    for (int d2 = 0; d2 < 2; ++d2) {
      const int dt = 2 * wave + d2;
      f32x4 acc = {0.f, 0.f, 0.f, 0.f};
#pragma unroll
      for (int w = 0; w < 4; ++w) { f32x4 v = *(lds_f32x4_ptr)(uintptr_t)(w * 8192 + dt * 1024 + l2 * 16); acc += v * sc[w]; }
      u32x2 wv = {cvtpk(acc[0] * inv, acc[1] * inv), cvtpk(acc[2] * inv, acc[3] * inv)};
      *(u32x2*)(og + dt * 32) = wv;
    }
  }
}

constexpr int K0_XT = 144 * 128;
constexpr int K0_WT = 64 * 512;
constexpr int K0_ST = K0_XT + K0_WT;
constexpr int K0_NS = 3;
constexpr int K0_LDS = K0_NS * K0_ST;
constexpr int K0_STEPS = DM / 64;

__global__ __launch_bounds__(1024, 4)
void mha_qproj_kernel(const __hip_bfloat16* __restrict__ xA, const __hip_bfloat16* __restrict__ wqA,
                      const __hip_bfloat16* __restrict__ xB, const __hip_bfloat16* __restrict__ wqB, __hip_bfloat16* __restrict__ qws) {
  const int tid = threadIdx.x;
  const int wave = __builtin_amdgcn_readfirstlane(tid >> 6);
  const int lane = tid & 63;
  const int r16 = lane & 15, g = lane >> 4, r32 = lane & 31, hh = lane >> 5, blk = g & 1, qq = r16 >> 2, pp = r16 & 3;
  const int bid = blockIdx.x;
  const int xcd = bid & 7, loc = bid >> 3;
  const int prob = xcd & 1, hp = ((xcd >> 1) & 1) * 8 + (loc & 7), b = (xcd >> 2) * 4 + (loc >> 3);
  if (wave >= 8) {
    const int lw = wave - 8;
    const char* xg = (const char*)(prob ? xB : xA) + (size_t)(b * SQ) * (DM * 2);
    const char* wg = (const char*)(prob ? wqB : wqA) + (size_t)hp * 512;
    unsigned offX[3], ldX[3], offW[4];
#pragma unroll
    for (int j = 0; j < 3; ++j) {
      int p = lw * 3 + j; p = p < 18 ? p : 17;
      const int row = p * 8 + (lane >> 3);
      offX[j] = row * (DM * 2) + (((lane & 7) ^ ((row >> 1) & 7)) << 4);
      ldX[j] = p * 1024;
    }
#pragma unroll
    for (int j = 0; j < 4; ++j) {
      const int p = lw * 4 + j;
      const int row = (p & 15) * 4 + g;
      const int fV = ((row & 3) << 2) | ((((row >> 2) ^ (row >> 3)) & 1) << 1);
      offW[j] = row * (HD * 2) + (p >> 4) * 256 + ((r16 ^ fV) << 4);
    }
#define K0_ISSUE(T, SL) do { const char* gx = xg + (T) * 128; const char* gw = wg + (size_t)(T) * 64 * (HD * 2); const unsigned lb = (SL) * K0_ST; \
    _Pragma("unroll") for (int j_ = 0; j_ < 3; ++j_) dma16(lb + __builtin_amdgcn_readfirstlane(ldX[j_]), offX[j_], gx); \
    _Pragma("unroll") for (int j_ = 0; j_ < 4; ++j_) dma16(lb + K0_XT + (lw * 4 + j_) * 1024, offW[j_], gw); } while (0)
    K0_ISSUE(0, 0); K0_ISSUE(1, 1);
    int slot = 0;
#pragma unroll 1
    for (int t = 0; t < K0_STEPS; ++t) {
      if (t + 1 < K0_STEPS) wait_vm<7>(); else wait_vm<0>();
      bar();
      if (t + 2 < K0_STEPS) { int s2 = slot + 2; if (s2 >= K0_NS) s2 -= K0_NS; K0_ISSUE(t + 2, s2); }
      slot = (slot + 1 == K0_NS) ? 0 : slot + 1;
    }
    return;
  }
  const int hs = wave >> 2, dq = wave & 3;
  const unsigned aX  = r32 * 128 + ((hh ^ ((r32 >> 1) & 7)) << 4);
  const unsigned aX8 = (128 + r16) * 128 + ((g ^ ((r16 >> 1) & 7)) << 4);
  const unsigned aW  = K0_XT + hs * 16384 + (((8 * hh + qq) * 256 + (qq << 6) + ((blk ^ hh) << 5) + ((pp >> 1) << 4) + ((pp & 1) << 3)) ^ ((unsigned)dq << 6));
  const unsigned aW8 = K0_XT + hs * 16384 + (((8 * g + qq) * 256 + (qq << 6) + ((g & 1) << 5) + ((pp >> 1) << 4) + ((pp & 1) << 3)) ^ ((unsigned)dq << 6));
  f32x16 acc[4]; f32x4 a8[2];
#pragma unroll
  for (int i = 0; i < 4; ++i)
#pragma unroll
    for (int r = 0; r < 16; ++r) acc[i][r] = 0.f;
  a8[0] = f32x4{0.f, 0.f, 0.f, 0.f}; a8[1] = f32x4{0.f, 0.f, 0.f, 0.f};
  int slot = 0;
#pragma unroll 1
  for (int t = 0; t < K0_STEPS; ++t) {
    bar();
    const unsigned sb = slot * K0_ST;
    s16x8 wf[4], w8[2][2], x8[2];
#pragma unroll
    for (int kk = 0; kk < 4; ++kk) { const unsigned a = aW + sb + kk * 4096; wf[kk] = lds_tr2(a, (a ^ 32u) + 1024); }
#pragma unroll
    for (int k2 = 0; k2 < 2; ++k2) {
      x8[k2] = lds_b128((aX8 ^ (k2 << 6)) + sb);
#pragma unroll
      for (int d2 = 0; d2 < 2; ++d2) { const unsigned a = (aW8 ^ (d2 << 5)) + sb + k2 * 8192; w8[k2][d2] = lds_tr2(a, (a ^ 32u) + 1024); }
    }
#pragma unroll
    for (int kk = 0; kk < 4; ++kk) {
      s16x8 xf[4];
#pragma unroll
      for (int mt = 0; mt < 4; ++mt) xf[mt] = lds_b128((aX ^ (kk << 5)) + sb + mt * 4096);
#pragma unroll
      for (int mt = 0; mt < 4; ++mt) acc[mt] = mfma32(wf[kk], xf[mt], acc[mt]);
    }
#pragma unroll
    for (int k2 = 0; k2 < 2; ++k2)
#pragma unroll
      for (int d2 = 0; d2 < 2; ++d2) a8[d2] = mfma16(w8[k2][d2], x8[k2], a8[d2]);
    slot = (slot + 1 == K0_NS) ? 0 : slot + 1;
  }
  char* qb = (char*)qws + ((size_t)(prob * MROWS + b * SQ) * HD + (hp * 2 + hs) * DH + dq * 32) * 2;
#pragma unroll
  for (int mt = 0; mt < 4; ++mt) {
    char* qp = qb + (size_t)(mt * 32 + r32) * (HD * 2) + hh * 16;
#pragma unroll
    for (int s2 = 0; s2 < 2; ++s2) {
      const f32x16& a = acc[mt]; const int b0 = 8 * s2;
      u32x4 w = pack_swap(a[b0] * SC_L2E, a[b0 + 1] * SC_L2E, a[b0 + 2] * SC_L2E, a[b0 + 3] * SC_L2E, a[b0 + 4] * SC_L2E, a[b0 + 5] * SC_L2E, a[b0 + 6] * SC_L2E, a[b0 + 7] * SC_L2E);
      *(u32x4*)(qp + s2 * 32) = w;
    }
  }
#pragma unroll
  for (int d2 = 0; d2 < 2; ++d2) {
    u32x2 w = {cvtpk(a8[d2][0] * SC_L2E, a8[d2][1] * SC_L2E), cvtpk(a8[d2][2] * SC_L2E, a8[d2][3] * SC_L2E)};
    *(u32x2*)(qb + (size_t)(128 + r16) * (HD * 2) + d2 * 32 + g * 8) = w;
  }
}

constexpr int K2_BM = 96, K2_BN = 64, K2_BK = 128;
constexpr int K2_OT = K2_BM * K2_BK * 2;
constexpr int K2_WT = K2_BK * K2_BN * 2;
constexpr int K2_ST = K2_OT + K2_WT;
#define K2NS 4
constexpr int K2_NS = K2NS;
constexpr int K2_LDS = K2_NS * K2_ST;
constexpr int K2_STEPS = HD / K2_BK;
constexpr int K2_NLD = 8;
constexpr int K2_THREADS = (8 + K2_NLD) * 64;

__global__ __launch_bounds__(K2_THREADS, 4)
void mha_oproj_kernel(const __hip_bfloat16* __restrict__ ows, const __hip_bfloat16* __restrict__ woA,
                      const __hip_bfloat16* __restrict__ woB, __hip_bfloat16* __restrict__ out) {
  const int tid = threadIdx.x;
  const int wave = __builtin_amdgcn_readfirstlane(tid >> 6);
  const int lane = tid & 63;
  const int r16 = lane & 15, g = lane >> 4;
  const int bid = blockIdx.x;
  const int xcd = bid & 7, loc = bid >> 3;
  const int mblk = xcd * 3 + loc / 10, nblk = loc % 10;
  const int prob = mblk / 12, mb = mblk % 12;
  if (wave >= 8) {
    const int lw = wave - 8;
    const char* og = (const char*)ows + (size_t)(prob * MROWS + mb * K2_BM) * (HD * 2);
    const char* wg = (const char*)(prob ? woB : woA) + (size_t)nblk * (K2_BN * 2);
    const int rot = 0;
    unsigned offO[3], offWo[2];
#pragma unroll
    for (int j = 0; j < 3; ++j) { const int row = lw * 12 + j * 4 + g; offO[j] = row * (HD * 2) + ((r16 ^ (row & 15)) << 4); }
#pragma unroll
    for (int j = 0; j < 2; ++j) {
      const int row = lw * 16 + j * 8 + (lane >> 3);
      offWo[j] = row * (DM * 2) + (((lane & 7) ^ (((row >> 1) & 1) << 2)) << 4);
    }
#define K2_OSAME 0
#define K2_WSAME 0
#define K2_ISSUE(T, SL) do { const char* go = og + (size_t)(K2_OSAME ? 0 : (T)) * (K2_BK * 2); const char* gw = wg + (size_t)(K2_WSAME ? 0 : (T)) * K2_BK * (DM * 2); \
    const unsigned lb = (SL) * K2_ST; \
    _Pragma("unroll") for (int j_ = 0; j_ < 3; ++j_) dma16(lb + lw * 3072 + j_ * 1024, offO[j_], go); \
    _Pragma("unroll") for (int j_ = 0; j_ < 2; ++j_) dma16(lb + K2_OT + lw * 2048 + j_ * 1024, offWo[j_], gw); } while (0)
#pragma unroll
    for (int i = 0; i < K2_NS - 1; ++i) K2_ISSUE((i + rot) & 31, i);
    int slot = 0;
#pragma unroll 1
    for (int t = 0; t < K2_STEPS; ++t) {
      if (t + K2_NS - 2 < K2_STEPS) wait_vm<5 * (K2_NS - 2)>();
      else if (K2_NS > 3 && t + K2_NS - 3 < K2_STEPS) wait_vm<(K2_NS > 3 ? 5 * (K2_NS - 3) : 0)>();
      else wait_vm<0>();
      bar();
      if (t + K2_NS - 1 < K2_STEPS) { int s2 = slot + K2_NS - 1; if (s2 >= K2_NS) s2 -= K2_NS; K2_ISSUE((t + K2_NS - 1 + rot) & 31, s2); }
      slot = (slot + 1 == K2_NS) ? 0 : slot + 1;
    }
    bar(); bar();
    return;
  }
  const int kq = wave >> 1, wn = wave & 1;
  const int r32 = lane & 31, hh = lane >> 5, blk = g & 1, qq = r16 >> 2, pp = r16 & 3;
  const unsigned aO = r32 * 256 + (((4 * kq + hh) ^ (r32 & 15)) << 4);
  unsigned aWo;
  {
    const int rowl = 8 * hh + qq;
    aWo = K2_OT + (32 * kq + rowl) * 128 + (((4 * wn + 2 * blk + (pp >> 1)) ^ (((rowl >> 1) & 1) << 2)) << 4) + ((pp & 1) << 3);
  }
  f32x16 acc[3];
#pragma unroll
  for (int i = 0; i < 3; ++i)
#pragma unroll
    for (int r = 0; r < 16; ++r) acc[i][r] = 0.f;
  int slot = 0;
  s16x8 wfA[2], ofA[2][3], wfB[2], ofB[2][3];
#define K2_READ(WF, OF) do { const unsigned sb = slot * K2_ST; \
    _Pragma("unroll") for (int s2 = 0; s2 < 2; ++s2) { const unsigned aw = aWo + sb + s2 * 16 * 128; WF[s2] = lds_tr2(aw, aw + 512); \
      const unsigned ao = (aO ^ (s2 << 5)) + sb; _Pragma("unroll") for (int mt = 0; mt < 3; ++mt) OF[s2][mt] = lds_b128(ao + mt * 8192); } \
    slot = (slot + 1 == K2_NS) ? 0 : slot + 1; } while (0)
#define K2_MMA(WF, OF) do { _Pragma("unroll") for (int s2 = 0; s2 < 2; ++s2) _Pragma("unroll") for (int mt = 0; mt < 3; ++mt) acc[mt] = mfma32(WF[s2], OF[s2][mt], acc[mt]); } while (0)
  bar(); K2_READ(wfA, ofA);
#pragma unroll 1
  for (int t = 1; t < K2_STEPS - 1; t += 2) {
    bar(); K2_READ(wfB, ofB); __builtin_amdgcn_sched_barrier(0); K2_MMA(wfA, ofA); __builtin_amdgcn_sched_barrier(0);
    bar(); K2_READ(wfA, ofA); __builtin_amdgcn_sched_barrier(0); K2_MMA(wfB, ofB); __builtin_amdgcn_sched_barrier(0);
  }
  bar(); K2_READ(wfB, ofB); __builtin_amdgcn_sched_barrier(0); K2_MMA(wfA, ofA); __builtin_amdgcn_sched_barrier(0);
  K2_MMA(wfB, ofB);
  {
    typedef __attribute__((address_space(3))) f32x4* lds_f32x4_ptr;
    bar();
    if (kq > 0) {
#pragma unroll
      for (int mt = 0; mt < 3; ++mt)
#pragma unroll
        for (int q4 = 0; q4 < 4; ++q4)
          *(lds_f32x4_ptr)(uintptr_t)((((kq - 1) * 2 + wn) * 12 + mt * 4 + q4) * 1024 + lane * 16) = f32x4{acc[mt][4 * q4], acc[mt][4 * q4 + 1], acc[mt][4 * q4 + 2], acc[mt][4 * q4 + 3]};
    }
    bar();
    if (kq == 0) {
#pragma unroll
      for (int mt = 0; mt < 3; ++mt) {
#pragma unroll
        for (int q4 = 0; q4 < 4; ++q4)
#pragma unroll
          for (int k2 = 0; k2 < 3; ++k2) {
            f32x4 v = *(lds_f32x4_ptr)(uintptr_t)(((k2 * 2 + wn) * 12 + mt * 4 + q4) * 1024 + lane * 16);
            acc[mt][4 * q4] += v[0]; acc[mt][4 * q4 + 1] += v[1]; acc[mt][4 * q4 + 2] += v[2]; acc[mt][4 * q4 + 3] += v[3];
          }
        const int row = mb * K2_BM + mt * 32 + r32;
        char* op = (char*)out + ((size_t)row * (2 * DM) + prob * DM + nblk * K2_BN + wn * 32 + hh * 8) * 2;
#pragma unroll
        for (int s2 = 0; s2 < 2; ++s2) {
          const int b0 = 8 * s2;
          u32x4 w = pack_swap(acc[mt][b0], acc[mt][b0 + 1], acc[mt][b0 + 2], acc[mt][b0 + 3], acc[mt][b0 + 4], acc[mt][b0 + 5], acc[mt][b0 + 6], acc[mt][b0 + 7]);
          *(u32x4*)(op + s2 * 32) = w;
        }
      }
    }
  }
}

extern "C" void kernel_launch(void* const* d_in, const int* in_sizes, int n_in,
                              void* d_out, int out_size, void* d_ws, size_t ws_size,
                              hipStream_t stream) {
  const __hip_bfloat16* xA  = (const __hip_bfloat16*)d_in[0];
  const __hip_bfloat16* kA  = (const __hip_bfloat16*)d_in[1];
  const __hip_bfloat16* vA  = (const __hip_bfloat16*)d_in[2];
  const __hip_bfloat16* wqA = (const __hip_bfloat16*)d_in[3];
  const __hip_bfloat16* woA = (const __hip_bfloat16*)d_in[4];
  const __hip_bfloat16* xB  = (const __hip_bfloat16*)d_in[5];
  const __hip_bfloat16* kB  = (const __hip_bfloat16*)d_in[6];
  const __hip_bfloat16* vB  = (const __hip_bfloat16*)d_in[7];
  const __hip_bfloat16* wqB = (const __hip_bfloat16*)d_in[8];
  const __hip_bfloat16* woB = (const __hip_bfloat16*)d_in[9];
  __hip_bfloat16* ows = (__hip_bfloat16*)d_ws;
  __hip_bfloat16* qws = (__hip_bfloat16*)((char*)d_ws + (size_t)32 * 1024 * 1024);
  (void)hipFuncSetAttribute((const void*)mha_qproj_kernel, hipFuncAttributeMaxDynamicSharedMemorySize, K0_LDS);
  (void)hipFuncSetAttribute((const void*)mha_attn_kernel, hipFuncAttributeMaxDynamicSharedMemorySize, K1_LDS);
  (void)hipFuncSetAttribute((const void*)mha_oproj_kernel, hipFuncAttributeMaxDynamicSharedMemorySize, K2_LDS);
  hipLaunchKernelGGL(mha_qproj_kernel, dim3(256), dim3(1024), K0_LDS, stream, xA, wqA, xB, wqB, qws);
  hipLaunchKernelGGL(mha_attn_kernel, dim3(512), dim3(K1_WAVES * 64), K1_LDS, stream, kA, vA, kB, vB, woA, woB, (const __hip_bfloat16*)qws, ows);
  hipLaunchKernelGGL(mha_oproj_kernel, dim3(240), dim3(K2_THREADS), K2_LDS, stream, (const __hip_bfloat16*)ows, woA, woB, (__hip_bfloat16*)d_out);
}
```

```cpp
#include <hip/hip_runtime.h>
#include <hip/hip_bf16.h>
#include <stdint.h>

typedef short    s16x4 __attribute__((ext_vector_type(4)));
typedef short    s16x8 __attribute__((ext_vector_type(8)));
typedef float    f32x4 __attribute__((ext_vector_type(4)));
typedef float    f32x2 __attribute__((ext_vector_type(2)));
typedef __bf16   bf16x2 __attribute__((ext_vector_type(2)));
typedef unsigned u32x4 __attribute__((ext_vector_type(4)));
typedef unsigned u32x2 __attribute__((ext_vector_type(2)));
typedef __attribute__((address_space(3))) s16x4* lds_s16x4_ptr;
typedef __attribute__((address_space(3))) const s16x8* lds_s16x8_ptr;
#define DI __device__ __forceinline__

constexpr int NB = 8, SQ = 144, DM = 640, NH = 32, SK = 1024, DH = 128;
constexpr int HD = NH * DH;
constexpr int MROWS = NB * SQ;

DI unsigned cvtpk(float lo, float hi) {
  f32x2 v = {lo, hi};
  bf16x2 b = __builtin_convertvector(v, bf16x2);
  return __builtin_bit_cast(unsigned, b);
}
DI f32x4 mfma16(s16x8 a, s16x8 b, f32x4 c) {
  return __builtin_amdgcn_mfma_f32_16x16x32_bf16(a, b, c, 0, 0, 0);
}
DI void dma16(unsigned lds_addr, unsigned voff, const char* gbase) {
  asm volatile("s_mov_b32 m0, %0\n\ts_nop 0\n\tglobal_load_lds_dwordx4 %1, %2"
               :: "s"(lds_addr), "v"(voff), "s"(gbase) : "memory");
}
DI void dma16nt(unsigned lds_addr, unsigned voff, const char* gbase) {
  asm volatile("s_mov_b32 m0, %0\n\ts_nop 0\n\tglobal_load_lds_dwordx4 %1, %2 nt"
               :: "s"(lds_addr), "v"(voff), "s"(gbase) : "memory");
}
template <int N> DI void wait_vm() { asm volatile("s_waitcnt vmcnt(%0)" :: "i"(N) : "memory"); }
DI void bar() { asm volatile("s_waitcnt lgkmcnt(0)\n\ts_barrier" ::: "memory"); }
DI s16x8 lds_b128(unsigned addr) { return *(lds_s16x8_ptr)(uintptr_t)addr; }
DI s16x8 lds_tr2(unsigned addr0, unsigned addr1) {
  s16x4 lo = __builtin_amdgcn_ds_read_tr16_b64_v4i16((lds_s16x4_ptr)(uintptr_t)addr0);
  s16x4 hi = __builtin_amdgcn_ds_read_tr16_b64_v4i16((lds_s16x4_ptr)(uintptr_t)addr1);
  return __builtin_shufflevector(lo, hi, 0, 1, 2, 3, 4, 5, 6, 7);
}
DI float xmax16_32(float v) {
  auto r = __builtin_amdgcn_permlane32_swap(__float_as_uint(v), __float_as_uint(v), false, false);
  v = fmaxf(__uint_as_float(r[0]), __uint_as_float(r[1]));
  auto r2 = __builtin_amdgcn_permlane16_swap(__float_as_uint(v), __float_as_uint(v), false, false);
  return fmaxf(__uint_as_float(r2[0]), __uint_as_float(r2[1]));
}
DI float xsum16_32(float v) {
  auto r = __builtin_amdgcn_permlane32_swap(__float_as_uint(v), __float_as_uint(v), false, false);
  v = __uint_as_float(r[0]) + __uint_as_float(r[1]);
  auto r2 = __builtin_amdgcn_permlane16_swap(__float_as_uint(v), __float_as_uint(v), false, false);
  return __uint_as_float(r2[0]) + __uint_as_float(r2[1]);
}

typedef float f32x16 __attribute__((ext_vector_type(16)));
constexpr int K1_WAVES = 4;
constexpr int K1_NSLOT = 5;
constexpr int K1_TILE  = 16384;
constexpr int K1_NWS   = 10;
constexpr int K1_NW    = 20;
constexpr int K1_LDS   = K1_NSLOT * K1_TILE;
constexpr float SC_L2E = 0.088388347648318447f * 1.4426950408889634f;
constexpr float K1_THR = 8.0f;

DI f32x4 mfma16k16(s16x4 a, s16x4 b, f32x4 c) { return __builtin_amdgcn_mfma_f32_16x16x16bf16_1k(a, b, c, 0, 0, 0); }
DI f32x16 mfma32(s16x8 a, s16x8 b, f32x16 c) { return __builtin_amdgcn_mfma_f32_32x32x16_bf16(a, b, c, 0, 0, 0); }
DI s16x4 lds_tr(unsigned addr) { return __builtin_amdgcn_ds_read_tr16_b64_v4i16((lds_s16x4_ptr)(uintptr_t)addr); }
DI float max3f(float a, float b, float c) { float r; asm("v_max3_f32 %0, %1, %2, %3" : "=v"(r) : "v"(a), "v"(b), "v"(c)); return r; }
DI float max2f(float a, float b) { float r; asm("v_max_f32_e32 %0, %1, %2" : "=v"(r) : "v"(a), "v"(b)); return r; }
DI float xmax32(float v) {
  auto r = __builtin_amdgcn_permlane32_swap(__float_as_uint(v), __float_as_uint(v), false, false);
  return max2f(__uint_as_float(r[0]), __uint_as_float(r[1]));
}
DI float xmax16(float v) {
  auto r = __builtin_amdgcn_permlane16_swap(__float_as_uint(v), __float_as_uint(v), false, false);
  return max2f(__uint_as_float(r[0]), __uint_as_float(r[1]));
}
DI float xsum32(float v) {
  auto r = __builtin_amdgcn_permlane32_swap(__float_as_uint(v), __float_as_uint(v), false, false);
  return __uint_as_float(r[0]) + __uint_as_float(r[1]);
}
DI u32x4 pack_swap(float a0, float a1, float a2, float a3, float b0, float b1, float b2, float b3) {
  unsigned A0 = cvtpk(a0, a1), A1 = cvtpk(a2, a3), B0 = cvtpk(b0, b1), B1 = cvtpk(b2, b3);
  auto r0 = __builtin_amdgcn_permlane32_swap(A0, B0, false, false);
  auto r1 = __builtin_amdgcn_permlane32_swap(A1, B1, false, false);
  return u32x4{r0[0], r1[0], r0[1], r1[1]};
}

#define STAMP(i) do {} while (0)
struct K1State {
  unsigned offK, offV;
  const char *kg, *vg;
  unsigned ldsw;
  unsigned aK, aV, aK8, aV8;
  int rot, wave;
  s16x8 q[8], q8[4];
  f32x16 o[4]; f32x4 o8[8];
  f32x16 negm; f32x4 negm8;
  float m, l, m8, l8;
  s16x8 p[4];
  s16x4 p8;
};

DI int k1_phys(int lg) { return (0x40321 >> (4 * lg)) & 7; }
DI void k1_issue_piece(K1State& st, int n, int slot, int i) {
  const int t = ((n >> 1) + st.rot) & 15;
  const unsigned lb = slot * K1_TILE + st.ldsw + i * 1024;
  if (n & 1) dma16nt(lb, st.offV ^ ((((i & 1) ^ (i >> 1))) << 5), st.vg + (size_t)t * K1_TILE + i * 1024);
  else       dma16nt(lb, st.offK ^ (i << 6), st.kg + (size_t)t * K1_TILE + i * 1024);
}
DI void k1_issue_kv(K1State& st, int n, int slot) {
#pragma unroll
  for (int i = 0; i < 4; ++i) k1_issue_piece(st, n, slot, i);
}

DI void k1_kstep(K1State& st, int slot, bool FIRST, bool ISSUE, int jn, int sn) {
  unsigned a0 = st.aK + slot * K1_TILE, a8 = st.aK8 + slot * K1_TILE;
  asm volatile("" : "+v"(a0), "+v"(a8));
  f32x16 s[2]; f32x4 s8;
  s16x8 kf[3][2], k8[2];
#pragma unroll
  for (int i = 0; i < 2; ++i) { kf[i][0] = lds_b128(a0 ^ (i << 5)); kf[i][1] = lds_b128((a0 ^ (i << 5)) + 8192); }
  k8[0] = lds_b128(a8);
#pragma unroll
  for (int sg = 0; sg < 8; ++sg) {
    if (sg < 6) { kf[(sg + 2) % 3][0] = lds_b128(a0 ^ ((sg + 2) << 5)); kf[(sg + 2) % 3][1] = lds_b128((a0 ^ ((sg + 2) << 5)) + 8192); }
    if ((sg & 1) == 0 && sg < 6) k8[((sg >> 1) + 1) & 1] = lds_b128(a8 ^ (((sg >> 1) + 1) << 6));
    if (sg == 0) {
      s[0] = mfma32(kf[0][0], st.q[0], st.negm);
      s[1] = mfma32(kf[0][1], st.q[0], st.negm);
      s8 = mfma16(k8[0], st.q8[0], st.negm8);
    } else {
      s[0] = mfma32(kf[sg % 3][0], st.q[sg], s[0]);
      s[1] = mfma32(kf[sg % 3][1], st.q[sg], s[1]);
      if ((sg & 1) == 0) s8 = mfma16(k8[(sg >> 1) & 1], st.q8[sg >> 1], s8);
    }
    __builtin_amdgcn_sched_barrier(0);
    if (ISSUE && st.wave == (sg >> 1)) { k1_issue_piece(st, jn, sn, 2 * (sg & 1)); k1_issue_piece(st, jn, sn, 2 * (sg & 1) + 1); }
    __builtin_amdgcn_sched_barrier(0);
  }
  STAMP(1);
  float t0 = max3f(s[0][0], s[0][1], s[0][2]), t1 = max3f(s[1][0], s[1][1], s[1][2]);
#pragma unroll
  for (int r = 3; r < 15; r += 2) { t0 = max3f(t0, s[0][r], s[0][r + 1]); t1 = max3f(t1, s[1][r], s[1][r + 1]); }
  float tm = max3f(t0, t1, max2f(s[0][15], s[1][15]));
  tm = xmax32(tm);
  float t8 = max2f(max2f(s8[0], s8[1]), max2f(s8[2], s8[3]));
  t8 = xmax16(xmax32(t8));
  if (FIRST || __any((tm > K1_THR) | (t8 > K1_THR))) {
    const float dl = FIRST ? tm : max2f(tm, 0.f);
    const float d8 = FIRST ? t8 : max2f(t8, 0.f);
    st.m += dl; st.m8 += d8;
#pragma unroll
    for (int r = 0; r < 16; ++r) { s[0][r] -= dl; s[1][r] -= dl; st.negm[r] = -st.m; }
#pragma unroll
    for (int r = 0; r < 4; ++r) { s8[r] -= d8; st.negm8[r] = -st.m8; }
    {
      const float f = FIRST ? 0.f : __builtin_amdgcn_exp2f(-dl), f8 = FIRST ? 0.f : __builtin_amdgcn_exp2f(-d8);
      st.l *= f; st.l8 *= f8;
#pragma unroll
      for (int dt = 0; dt < 4; ++dt)
#pragma unroll
        for (int r = 0; r < 16; ++r) st.o[dt][r] *= f;
#pragma unroll
      for (int dt = 0; dt < 8; ++dt)
#pragma unroll
        for (int r = 0; r < 4; ++r) st.o8[dt][r] *= f8;
    }
  }
  float ps0 = 0.f, ps1 = 0.f, ps8 = 0.f;
#pragma unroll
  for (int r = 0; r < 16; ++r) {
    float e0 = __builtin_amdgcn_exp2f(s[0][r]), e1 = __builtin_amdgcn_exp2f(s[1][r]);
    s[0][r] = e0; s[1][r] = e1; ps0 += e0; ps1 += e1;
  }
#pragma unroll
  for (int r = 0; r < 4; ++r) { float e = __builtin_amdgcn_exp2f(s8[r]); s8[r] = e; ps8 += e; }
  st.l += ps0 + ps1; st.l8 += ps8;
#pragma unroll
  for (int ks = 0; ks < 4; ++ks) {
    const int kt = ks >> 1, b = 8 * (ks & 1);
    u32x4 w = {cvtpk(s[kt][b], s[kt][b + 1]), cvtpk(s[kt][b + 2], s[kt][b + 3]), cvtpk(s[kt][b + 4], s[kt][b + 5]), cvtpk(s[kt][b + 6], s[kt][b + 7])};
    st.p[ks] = __builtin_bit_cast(s16x8, w);
  }
  u32x2 w8 = {cvtpk(s8[0], s8[1]), cvtpk(s8[2], s8[3])};
  st.p8 = __builtin_bit_cast(s16x4, w8);
}

DI void k1_vstep(K1State& st, int slot, bool ISSUE, int jn, int sn) {
  unsigned a0 = st.aV + slot * K1_TILE, a8 = st.aV8 + slot * K1_TILE;
  asm volatile("" : "+v"(a0), "+v"(a8));
  s16x8 vf[2][4];
#pragma unroll
  for (int i = 0; i < 4; ++i) { const unsigned a = a0 ^ (i << 6); vf[0][i] = lds_tr2(a, (a ^ 32u) + 2048); }
#pragma unroll
  for (int ks = 0; ks < 4; ++ks) {
    if (ks < 3) {
#pragma unroll
      for (int i = 0; i < 4; ++i) { const unsigned a = (a0 ^ (i << 6)) + (ks + 1) * 4096; vf[(ks + 1) & 1][i] = lds_tr2(a, (a ^ 32u) + 2048); }
    } else {
#pragma unroll
      for (int i = 0; i < 4; ++i) vf[0][i] = lds_tr2(a8 ^ ((2 * i) << 5), a8 ^ ((2 * i + 1) << 5));
    }
#pragma unroll
    for (int i = 0; i < 4; ++i) st.o[i] = mfma32(vf[ks & 1][i], st.p[ks], st.o[i]);
    __builtin_amdgcn_sched_barrier(0);
    if (ISSUE && st.wave == ks) k1_issue_kv(st, jn, sn);
    __builtin_amdgcn_sched_barrier(0);
  }
#pragma unroll
  for (int i = 0; i < 4; ++i) {
    s16x4 lo = __builtin_shufflevector(vf[0][i], vf[0][i], 0, 1, 2, 3), hi = __builtin_shufflevector(vf[0][i], vf[0][i], 4, 5, 6, 7);
    st.o8[2 * i] = mfma16k16(lo, st.p8, st.o8[2 * i]);
    st.o8[2 * i + 1] = mfma16k16(hi, st.p8, st.o8[2 * i + 1]);
  }
}

DI void k1_pair(K1State& st, int it, int& lg) {
  const int n = 2 * it;
  if (it == 0) wait_vm<8>(); else if (it < 15) wait_vm<12>(); else wait_vm<4>();
  bar();
  STAMP(0);
  if (it == 0) k1_issue_kv(st, 3, 0);
  int l4 = lg + 4; if (l4 >= 5) l4 -= 5;
  k1_kstep(st, k1_phys(lg), it == 0, it < 14, n + 4, k1_phys(l4));
  lg = (lg == 4) ? 0 : lg + 1;
  STAMP(2);
  if (it < 14) wait_vm<12>(); else if (it == 14) wait_vm<8>(); else wait_vm<0>();
  bar();
  STAMP(3);
  l4 = lg + 4; if (l4 >= 5) l4 -= 5;
  k1_vstep(st, k1_phys(lg), it < 14, n + 5, k1_phys(l4));
  lg = (lg == 4) ? 0 : lg + 1;
  STAMP(4);
}

DI s16x8 k1_qfrag16(const f32x4& t0, const f32x4& t1, int srcA, int srcB, bool hiT) {
  unsigned p00 = cvtpk(t0[0], t0[1]), p01 = cvtpk(t0[2], t0[3]), p10 = cvtpk(t1[0], t1[1]), p11 = cvtpk(t1[2], t1[3]);
  u32x4 w;
  { unsigned a0 = __builtin_amdgcn_ds_bpermute(srcA, p00), a1 = __builtin_amdgcn_ds_bpermute(srcA, p10); w[0] = hiT ? a1 : a0; }
  { unsigned a0 = __builtin_amdgcn_ds_bpermute(srcA, p01), a1 = __builtin_amdgcn_ds_bpermute(srcA, p11); w[1] = hiT ? a1 : a0; }
  { unsigned a0 = __builtin_amdgcn_ds_bpermute(srcB, p00), a1 = __builtin_amdgcn_ds_bpermute(srcB, p10); w[2] = hiT ? a1 : a0; }
  { unsigned a0 = __builtin_amdgcn_ds_bpermute(srcB, p01), a1 = __builtin_amdgcn_ds_bpermute(srcB, p11); w[3] = hiT ? a1 : a0; }
  return __builtin_bit_cast(s16x8, w);
}

__global__ __launch_bounds__(K1_WAVES * 64, 2)
void mha_attn_kernel(const __hip_bfloat16* __restrict__ xA, const __hip_bfloat16* __restrict__ kA,
                     const __hip_bfloat16* __restrict__ vA, const __hip_bfloat16* __restrict__ wqA,
                     const __hip_bfloat16* __restrict__ xB, const __hip_bfloat16* __restrict__ kB,
                     const __hip_bfloat16* __restrict__ vB, const __hip_bfloat16* __restrict__ wqB,
                     const __hip_bfloat16* __restrict__ woA, const __hip_bfloat16* __restrict__ woB,
                     __hip_bfloat16* __restrict__ ows) {
  const int tid = threadIdx.x;
  const int wave = __builtin_amdgcn_readfirstlane(tid >> 6);
  const int lane = tid & 63;
  const int r16 = lane & 15, g = lane >> 4, r32 = lane & 31, hh = lane >> 5, blk = g & 1;
  const int bid = blockIdx.x;
  const int xcd = bid & 7, loc = bid >> 3;
  const int prob = xcd & 1, h = ((xcd >> 1) & 1) * 16 + (loc & 15), b = (xcd >> 2) * 4 + (loc >> 4);

  const char* xg = (const char*)(prob ? xB : xA) + (size_t)(b * SQ) * (DM * 2);
  const char* wg = (const char*)(prob ? wqB : wqA) + (size_t)h * (DH * 2);
  K1State st;
  st.kg = (const char*)(prob ? kB : kA) + (size_t)(b * NH + h) * (SK * DH * 2);
  st.vg = (const char*)(prob ? vB : vA) + (size_t)(b * NH + h) * (SK * DH * 2);
  st.ldsw = wave * 4096; st.wave = wave;
  st.rot = (h + 5 * b) & 15;
  st.offK = (wave * 16 + g) * 256 + ((r16 ^ g) << 4);
  st.offV = (wave * 16 + g) * 256 + ((r16 ^ (g << 2)) << 4);
  const int qq = r16 >> 2, pp = r16 & 3;
  const unsigned pb = ((pp >> 1) << 4) + ((pp & 1) << 3);
  st.aK  = r32 * 256 + ((hh ^ (r32 & 15)) << 4);
  st.aK8 = (wave * 16 + r16) * 256 + ((g ^ r16) << 4);
  st.aV  = (4 * hh + qq) * 256 + (qq << 6) + ((blk ^ hh) << 5) + pb;
  st.aV8 = (wave * 16 + 4 * g + qq) * 256 + (qq << 6) + ((((g & 1) ^ (g >> 1))) << 5) + pb;
  const unsigned aW  = (8 * hh + qq) * 256 + (qq << 6) + ((blk ^ hh) << 5) + pb;
  const unsigned aW8 = ((8 * g + qq) * 256 + (qq << 6) + ((g & 1) << 5) + pb) ^ ((unsigned)wave << 6);
  const unsigned xoff8 = (128 + r16) * (DM * 2) + g * 16;
  unsigned offX[4];
#pragma unroll
  for (int j = 0; j < 4; ++j) {
    const int row = wave * 32 + j * 8 + (lane >> 3);
    offX[j] = row * (DM * 2) + (((lane & 7) ^ ((row >> 1) & 7)) << 4);
  }
  const unsigned aX = (wave * 32 + r32) * 128 + ((hh ^ ((r32 >> 1) & 7)) << 4);

  s16x8 xf8[3][2];
  u32x4 wr[4][4];
  unsigned offWg[4], aWw;
#pragma unroll
  for (int i = 0; i < 4; ++i) { if (i < 3) { xf8[i][0] = s16x8{0,0,0,0,0,0,0,0}; xf8[i][1] = s16x8{0,0,0,0,0,0,0,0}; }
#pragma unroll
    for (int j = 0; j < 4; ++j) wr[i][j] = u32x4{0u, 0u, 0u, 0u}; }
#pragma unroll
  for (int j = 0; j < 4; ++j) offWg[j] = (wave * 16 + j * 4 + g) * (HD * 2) + (r16 << 4);
  aWw = wave * 4096 + g * 256;
  typedef __attribute__((address_space(3))) u32x4* lds_u32x4_ptr;
#define X_LD(DST, OFF, IMM) asm volatile("global_load_dwordx4 %0, %1, %2 offset:%3" : "+v"(DST) : "v"(OFF), "s"(xg), "i"(IMM) : "memory")
#define X8_LOAD(T) do { X_LD(xf8[(T) % 3][0], xoff8, (T) * 128); X_LD(xf8[(T) % 3][1], xoff8, (T) * 128 + 64); } while (0)
#define XD_ISSUE(T) do { _Pragma("unroll") for (int i_ = 0; i_ < 4; ++i_) dma16(((T) % 3) * K1_TILE + st.ldsw + i_ * 1024, offX[i_], xg + (XSAME ? 0 : (T)) * 128); } while (0)
#define WSAME 0
#define XSAME 0
#define W_LD(T) do { const char* gs_ = wg + (size_t)(WSAME ? 0 : (T)) * 64 * (HD * 2); _Pragma("unroll") for (int i_ = 0; i_ < 4; ++i_) \
    asm volatile("global_load_dwordx4 %0, %1, %2" : "+v"(wr[(T) & 3][i_]) : "v"(offWg[i_]), "s"(gs_) : "memory"); } while (0)
#define W_PIN(T) asm volatile("" : "+v"(wr[(T) & 3][0]), "+v"(wr[(T) & 3][1]), "+v"(wr[(T) & 3][2]), "+v"(wr[(T) & 3][3]))
#define W_WRITE(T) do { _Pragma("unroll") for (int i_ = 0; i_ < 4; ++i_) \
    *(lds_u32x4_ptr)(uintptr_t)((3 + ((T) & 1)) * K1_TILE + aWw + i_ * 1024 + ((r16 ^ ((g << 2) | (((i_ & 1) ^ (i_ >> 1)) << 1))) << 4)) = wr[(T) & 3][i_]; } while (0)

  u32x4 wop[10];
#pragma unroll
  for (int i = 0; i < 10; ++i) wop[i] = u32x4{0u, 0u, 0u, 0u};
  if (bid >= 256) {
    const int c = bid - 256;
    const char* wo = (const char*)((c & 1) ? woB : woA) + (size_t)(c >> 1) * 40960;
#pragma unroll
    for (int i = 0; i < 10; ++i) asm volatile("global_load_dwordx4 %0, %1, %2" : "+v"(wop[i]) : "v"((unsigned)(tid * 16)), "s"(wo + i * 4096) : "memory");
  }
  W_LD(0); W_LD(1);
  W_LD(2); XD_ISSUE(0); X8_LOAD(0);
  W_LD(3); XD_ISSUE(1); X8_LOAD(1);

  f32x16 qa[4]; f32x4 qa8[2];
#pragma unroll
  for (int dt = 0; dt < 4; ++dt)
#pragma unroll
    for (int r = 0; r < 16; ++r) qa[dt][r] = 0.f;
  qa8[0] = f32x4{0.f, 0.f, 0.f, 0.f}; qa8[1] = f32x4{0.f, 0.f, 0.f, 0.f};

  wait_vm<24>();
  asm volatile("" : "+v"(wop[0]), "+v"(wop[1]), "+v"(wop[2]), "+v"(wop[3]), "+v"(wop[4]), "+v"(wop[5]), "+v"(wop[6]), "+v"(wop[7]), "+v"(wop[8]), "+v"(wop[9]));
  W_PIN(0); W_WRITE(0);
#pragma unroll
  for (int t = 0; t < K1_NWS; ++t) {
    if (t <= 6) wait_vm<10>(); else if (t <= 8) wait_vm<6>(); else wait_vm<4>();
    asm volatile("" : "+v"(xf8[t % 3][0]), "+v"(xf8[t % 3][1]));
    if (t + 1 < K1_NWS) W_PIN(t + 1);
    bar();
    STAMP(5);
    if (t + 1 < K1_NWS) W_WRITE(t + 1);
    unsigned a0 = aW + (3 + (t & 1)) * K1_TILE, a8 = aW8 + (3 + (t & 1)) * K1_TILE, ax = aX + (t % 3) * K1_TILE;
    asm volatile("" : "+v"(a0), "+v"(a8), "+v"(ax));
    {
      s16x8 wf[2][4], xf[4];
#pragma unroll
      for (int kk = 0; kk < 4; ++kk) xf[kk] = lds_b128(ax ^ (kk << 5));
#pragma unroll
      for (int i = 0; i < 4; ++i) { const unsigned a = a0 ^ (i << 6); wf[0][i] = lds_tr2(a, (a ^ 32u) + 1024); }
#pragma unroll
      for (int kk = 0; kk < 4; ++kk) {
        if (kk < 3) {
#pragma unroll
          for (int i = 0; i < 4; ++i) { const unsigned a = (a0 ^ (i << 6)) + (kk + 1) * 4096; wf[(kk + 1) & 1][i] = lds_tr2(a, (a ^ 32u) + 1024); }
        } else {
#pragma unroll
          for (int i = 0; i < 4; ++i) { const unsigned a = (a8 ^ ((i & 1) << 5)) + (i >> 1) * 8192; wf[0][i] = lds_tr2(a, (a ^ 32u) + 1024); }
        }
#pragma unroll
        for (int i = 0; i < 4; ++i) qa[i] = mfma32(wf[kk & 1][i], xf[kk], qa[i]);
        __builtin_amdgcn_sched_barrier(0);
        if (wave == kk) {
          if (t + 4 < K1_NWS) W_LD(t + 4);
          if (t + 2 < K1_NWS) XD_ISSUE(t + 2); else k1_issue_kv(st, t - 8, t == 8 ? 1 : 2);
          if (t + 2 < K1_NWS) X8_LOAD(t + 2);
          if (t == 9) k1_issue_kv(st, 2, 3);
        }
        __builtin_amdgcn_sched_barrier(0);
      }
#pragma unroll
      for (int i = 0; i < 4; ++i) qa8[i & 1] = mfma16(wf[0][i], xf8[t % 3][i >> 1], qa8[i & 1]);
    }
    STAMP(6);
  }
  {
#pragma unroll
    for (int s = 0; s < 8; ++s) {
      const f32x16& a = qa[s >> 1]; const int b0 = 8 * (s & 1);
      u32x4 w = pack_swap(a[b0] * SC_L2E, a[b0 + 1] * SC_L2E, a[b0 + 2] * SC_L2E, a[b0 + 3] * SC_L2E,
                          a[b0 + 4] * SC_L2E, a[b0 + 5] * SC_L2E, a[b0 + 6] * SC_L2E, a[b0 + 7] * SC_L2E);
      st.q[s] = __builtin_bit_cast(s16x8, w);
    }
    const int srcA = (r16 + 16 * (2 * (g & 1))) << 2, srcB = srcA + 64;
    const bool hiT = (g >> 1) != 0;
    s16x8 q8w = k1_qfrag16(qa8[0] * SC_L2E, qa8[1] * SC_L2E, srcA, srcB, hiT);
    const unsigned sc = 0;
    asm volatile("s_waitcnt lgkmcnt(0)" ::: "memory"); bar();
    *(__attribute__((address_space(3))) s16x8*)(uintptr_t)(sc + wave * 1024 + lane * 16) = q8w;
    asm volatile("s_waitcnt lgkmcnt(0)" ::: "memory"); bar();
#pragma unroll
    for (int s = 0; s < 4; ++s) st.q8[s] = lds_b128(sc + s * 1024 + lane * 16);
    asm volatile("s_waitcnt lgkmcnt(0)" ::: "memory");
  }
#pragma unroll
  for (int dt = 0; dt < 4; ++dt)
#pragma unroll
    for (int r = 0; r < 16; ++r) st.o[dt][r] = 0.f;
#pragma unroll
  for (int dt = 0; dt < 8; ++dt) st.o8[dt] = f32x4{0.f, 0.f, 0.f, 0.f};
#pragma unroll
  for (int r = 0; r < 16; ++r) st.negm[r] = 0.f;
  st.negm8 = f32x4{0.f, 0.f, 0.f, 0.f};
  st.m = st.m8 = 0.f; st.l = st.l8 = 0.f;

  int slot = 0;
#pragma unroll 1
  for (int it = 0; it < 16; ++it) k1_pair(st, it, slot);

  const int tz = __builtin_amdgcn_mbcnt_hi(~0u, __builtin_amdgcn_mbcnt_lo(~0u, 0u));
  char* ob = (char*)ows + ((size_t)(prob * MROWS + b * SQ) * HD + h * DH) * 2;
  {
    const float inv = 1.f / xsum32(st.l);
    char* og = ob + (size_t)(wave * 32 + (tz & 31)) * (HD * 2) + ((tz >> 5) & 1) * 16;
#pragma unroll
    for (int dt = 0; dt < 4; ++dt)
#pragma unroll
      for (int s2 = 0; s2 < 2; ++s2) {
        const f32x16& a = st.o[dt]; const int b0 = 8 * s2;
        u32x4 w = pack_swap(a[b0] * inv, a[b0 + 1] * inv, a[b0 + 2] * inv, a[b0 + 3] * inv, a[b0 + 4] * inv, a[b0 + 5] * inv, a[b0 + 6] * inv, a[b0 + 7] * inv);
        *(u32x4*)(og + dt * 64 + s2 * 32) = w;
      }
  }
  {
    typedef __attribute__((address_space(3))) f32x4* lds_f32x4_ptr;
    typedef __attribute__((address_space(3))) float* lds_f32_ptr;
    const int l2 = tz & 63, r = tz & 15;
    const float l8t = xsum16_32(st.l8);
    asm volatile("s_waitcnt lgkmcnt(0)" ::: "memory"); bar();
#pragma unroll
    for (int dt = 0; dt < 8; ++dt) *(lds_f32x4_ptr)(uintptr_t)(wave * 8192 + dt * 1024 + l2 * 16) = st.o8[dt];
    if (l2 < 16) { *(lds_f32_ptr)(uintptr_t)(32768 + (wave * 16 + r) * 4) = st.m8; *(lds_f32_ptr)(uintptr_t)(32768 + 256 + (wave * 16 + r) * 4) = l8t; }
    asm volatile("s_waitcnt lgkmcnt(0)" ::: "memory"); bar();
    float mw[4], lw[4];
#pragma unroll
    for (int w = 0; w < 4; ++w) { mw[w] = *(lds_f32_ptr)(uintptr_t)(32768 + (w * 16 + r) * 4); lw[w] = *(lds_f32_ptr)(uintptr_t)(32768 + 256 + (w * 16 + r) * 4); }
    const float M = fmaxf(fmaxf(mw[0], mw[1]), fmaxf(mw[2], mw[3]));
    float L = 0.f, sc[4];
#pragma unroll
    for (int w = 0; w < 4; ++w) { sc[w] = __builtin_amdgcn_exp2f(mw[w] - M); L += lw[w] * sc[w]; }
    const float inv = 1.f / L;
    char* og = ob + (size_t)(128 + r) * (HD * 2) + ((l2 >> 4) & 3) * 8;
#pragma unroll
    for (int d2 = 0; d2 < 2; ++d2) {
      const int dt = 2 * wave + d2;
      f32x4 acc = {0.f, 0.f, 0.f, 0.f};
#pragma unroll
      for (int w = 0; w < 4; ++w) { f32x4 v = *(lds_f32x4_ptr)(uintptr_t)(w * 8192 + dt * 1024 + l2 * 16); acc += v * sc[w]; }
      u32x2 wv = {cvtpk(acc[0] * inv, acc[1] * inv), cvtpk(acc[2] * inv, acc[3] * inv)};
      *(u32x2*)(og + dt * 32) = wv;
    }
  }
}

constexpr int K2_BM = 96, K2_BN = 64, K2_BK = 128;
constexpr int K2_OT = K2_BM * K2_BK * 2;
constexpr int K2_WT = K2_BK * K2_BN * 2;
constexpr int K2_ST = K2_OT + K2_WT;
#define K2NS 4
constexpr int K2_NS = K2NS;
constexpr int K2_LDS = K2_NS * K2_ST;
constexpr int K2_STEPS = HD / K2_BK;
constexpr int K2_NLD = 8;
constexpr int K2_THREADS = (8 + K2_NLD) * 64;

__global__ __launch_bounds__(K2_THREADS, 4)
void mha_oproj_kernel(const __hip_bfloat16* __restrict__ ows, const __hip_bfloat16* __restrict__ woA,
                      const __hip_bfloat16* __restrict__ woB, __hip_bfloat16* __restrict__ out) {
  const int tid = threadIdx.x;
  const int wave = __builtin_amdgcn_readfirstlane(tid >> 6);
  const int lane = tid & 63;
  const int r16 = lane & 15, g = lane >> 4;
  const int bid = blockIdx.x;
  const int xcd = bid & 7, loc = bid >> 3;
  const int mblk = xcd * 3 + loc / 10, nblk = loc % 10;
  const int prob = mblk / 12, mb = mblk % 12;
  if (wave >= 8) {
    const int lw = wave - 8;
    const char* og = (const char*)ows + (size_t)(prob * MROWS + mb * K2_BM) * (HD * 2);
    const char* wg = (const char*)(prob ? woB : woA) + (size_t)nblk * (K2_BN * 2);
    const int rot = 0;
    unsigned offO[3], offWo[2];
#pragma unroll
    for (int j = 0; j < 3; ++j) { const int row = lw * 12 + j * 4 + g; offO[j] = row * (HD * 2) + ((r16 ^ (row & 15)) << 4); }
#pragma unroll
    for (int j = 0; j < 2; ++j) {
      const int row = lw * 16 + j * 8 + (lane >> 3);
      offWo[j] = row * (DM * 2) + (((lane & 7) ^ (((row >> 1) & 1) << 2)) << 4);
    }
#define K2_OSAME 0
#define K2_WSAME 0
#define K2_ISSUE(T, SL) do { const char* go = og + (size_t)(K2_OSAME ? 0 : (T)) * (K2_BK * 2); const char* gw = wg + (size_t)(K2_WSAME ? 0 : (T)) * K2_BK * (DM * 2); \
    const unsigned lb = (SL) * K2_ST; \
    _Pragma("unroll") for (int j_ = 0; j_ < 3; ++j_) dma16(lb + lw * 3072 + j_ * 1024, offO[j_], go); \
    _Pragma("unroll") for (int j_ = 0; j_ < 2; ++j_) dma16(lb + K2_OT + lw * 2048 + j_ * 1024, offWo[j_], gw); } while (0)
#pragma unroll
    for (int i = 0; i < K2_NS - 1; ++i) K2_ISSUE((i + rot) & 31, i);
    int slot = 0;
#pragma unroll 1
    for (int t = 0; t < K2_STEPS; ++t) {
      if (t + K2_NS - 2 < K2_STEPS) wait_vm<5 * (K2_NS - 2)>();
      else if (K2_NS > 3 && t + K2_NS - 3 < K2_STEPS) wait_vm<(K2_NS > 3 ? 5 * (K2_NS - 3) : 0)>();
      else wait_vm<0>();
      bar();
      if (t + K2_NS - 1 < K2_STEPS) { int s2 = slot + K2_NS - 1; if (s2 >= K2_NS) s2 -= K2_NS; K2_ISSUE((t + K2_NS - 1 + rot) & 31, s2); }
      slot = (slot + 1 == K2_NS) ? 0 : slot + 1;
    }
    bar(); bar();
    return;
  }
  const int kq = wave >> 1, wn = wave & 1;
  const int r32 = lane & 31, hh = lane >> 5, blk = g & 1, qq = r16 >> 2, pp = r16 & 3;
  const unsigned aO = r32 * 256 + (((4 * kq + hh) ^ (r32 & 15)) << 4);
  unsigned aWo;
  {
    const int rowl = 8 * hh + qq;
    aWo = K2_OT + (32 * kq + rowl) * 128 + (((4 * wn + 2 * blk + (pp >> 1)) ^ (((rowl >> 1) & 1) << 2)) << 4) + ((pp & 1) << 3);
  }
  f32x16 acc[3];
#pragma unroll
  for (int i = 0; i < 3; ++i)
#pragma unroll
    for (int r = 0; r < 16; ++r) acc[i][r] = 0.f;
  int slot = 0;
  s16x8 wfA[2], ofA[2][3], wfB[2], ofB[2][3];
#define K2_READ(WF, OF) do { const unsigned sb = slot * K2_ST; \
    _Pragma("unroll") for (int s2 = 0; s2 < 2; ++s2) { const unsigned aw = aWo + sb + s2 * 16 * 128; WF[s2] = lds_tr2(aw, aw + 512); \
      const unsigned ao = (aO ^ (s2 << 5)) + sb; _Pragma("unroll") for (int mt = 0; mt < 3; ++mt) OF[s2][mt] = lds_b128(ao + mt * 8192); } \
    slot = (slot + 1 == K2_NS) ? 0 : slot + 1; } while (0)
#define K2_MMA(WF, OF) do { _Pragma("unroll") for (int s2 = 0; s2 < 2; ++s2) _Pragma("unroll") for (int mt = 0; mt < 3; ++mt) acc[mt] = mfma32(WF[s2], OF[s2][mt], acc[mt]); } while (0)
  bar(); K2_READ(wfA, ofA);
#pragma unroll 1
  for (int t = 1; t < K2_STEPS - 1; t += 2) {
    bar(); K2_READ(wfB, ofB); __builtin_amdgcn_sched_barrier(0); K2_MMA(wfA, ofA); __builtin_amdgcn_sched_barrier(0);
    bar(); K2_READ(wfA, ofA); __builtin_amdgcn_sched_barrier(0); K2_MMA(wfB, ofB); __builtin_amdgcn_sched_barrier(0);
  }
  bar(); K2_READ(wfB, ofB); __builtin_amdgcn_sched_barrier(0); K2_MMA(wfA, ofA); __builtin_amdgcn_sched_barrier(0);
  K2_MMA(wfB, ofB);
  {
    typedef __attribute__((address_space(3))) f32x4* lds_f32x4_ptr;
    bar();
    if (kq > 0) {
#pragma unroll
      for (int mt = 0; mt < 3; ++mt)
#pragma unroll
        for (int q4 = 0; q4 < 4; ++q4)
          *(lds_f32x4_ptr)(uintptr_t)((((kq - 1) * 2 + wn) * 12 + mt * 4 + q4) * 1024 + lane * 16) = f32x4{acc[mt][4 * q4], acc[mt][4 * q4 + 1], acc[mt][4 * q4 + 2], acc[mt][4 * q4 + 3]};
    }
    bar();
    if (kq == 0) {
#pragma unroll
      for (int mt = 0; mt < 3; ++mt) {
#pragma unroll
        for (int q4 = 0; q4 < 4; ++q4)
#pragma unroll
          for (int k2 = 0; k2 < 3; ++k2) {
            f32x4 v = *(lds_f32x4_ptr)(uintptr_t)(((k2 * 2 + wn) * 12 + mt * 4 + q4) * 1024 + lane * 16);
            acc[mt][4 * q4] += v[0]; acc[mt][4 * q4 + 1] += v[1]; acc[mt][4 * q4 + 2] += v[2]; acc[mt][4 * q4 + 3] += v[3];
          }
        const int row = mb * K2_BM + mt * 32 + r32;
        char* op = (char*)out + ((size_t)row * (2 * DM) + prob * DM + nblk * K2_BN + wn * 32 + hh * 8) * 2;
#pragma unroll
        for (int s2 = 0; s2 < 2; ++s2) {
          const int b0 = 8 * s2;
          u32x4 w = pack_swap(acc[mt][b0], acc[mt][b0 + 1], acc[mt][b0 + 2], acc[mt][b0 + 3], acc[mt][b0 + 4], acc[mt][b0 + 5], acc[mt][b0 + 6], acc[mt][b0 + 7]);
          *(u32x4*)(op + s2 * 32) = w;
        }
      }
    }
  }
}

extern "C" void kernel_launch(void* const* d_in, const int* in_sizes, int n_in,
                              void* d_out, int out_size, void* d_ws, size_t ws_size,
                              hipStream_t stream) {
  const __hip_bfloat16* xA  = (const __hip_bfloat16*)d_in[0];
  const __hip_bfloat16* kA  = (const __hip_bfloat16*)d_in[1];
  const __hip_bfloat16* vA  = (const __hip_bfloat16*)d_in[2];
  const __hip_bfloat16* wqA = (const __hip_bfloat16*)d_in[3];
  const __hip_bfloat16* woA = (const __hip_bfloat16*)d_in[4];
  const __hip_bfloat16* xB  = (const __hip_bfloat16*)d_in[5];
  const __hip_bfloat16* kB  = (const __hip_bfloat16*)d_in[6];
  const __hip_bfloat16* vB  = (const __hip_bfloat16*)d_in[7];
  const __hip_bfloat16* wqB = (const __hip_bfloat16*)d_in[8];
  const __hip_bfloat16* woB = (const __hip_bfloat16*)d_in[9];
  __hip_bfloat16* ows = (__hip_bfloat16*)d_ws;
  (void)hipFuncSetAttribute((const void*)mha_attn_kernel, hipFuncAttributeMaxDynamicSharedMemorySize, K1_LDS);
  (void)hipFuncSetAttribute((const void*)mha_oproj_kernel, hipFuncAttributeMaxDynamicSharedMemorySize, K2_LDS);
  hipLaunchKernelGGL(mha_attn_kernel, dim3(512), dim3(K1_WAVES * 64), K1_LDS, stream, xA, kA, vA, wqA, xB, kB, vB, wqB, woA, woB, ows);
  hipLaunchKernelGGL(mha_oproj_kernel, dim3(240), dim3(K2_THREADS), K2_LDS, stream, (const __hip_bfloat16*)ows, woA, woB, (__hip_bfloat16*)d_out);
}
```
